```python
import torch

def _softmax(x, axis=-1):
    return torch.softmax(x, dim=axis)
def _gelu(x):
    return torch.nn.functional.gelu(x, approximate="tanh")
def _silu(x):
    return torch.nn.functional.silu(x)
def _sigmoid(x):
    return torch.sigmoid(x)
def _layernorm(x, axis=-1, eps=1e-5):
    m = x.mean(dim=axis, keepdim=True)
    v = x.var(dim=axis, keepdim=True, unbiased=False)
    return (x - m) / torch.sqrt(v + eps)
def _scatter_add(tgt, idx, src, axis=0):
    return tgt.clone().index_add_(axis, idx, src)
def _segment_sum(src, seg, n):
    out = torch.zeros((n,) + src.shape[1:], dtype=src.dtype)
    return out.index_add_(0, seg, src)
def _pad(x, pw):
    flat = tuple(v for pair in reversed(pw) for v in pair)
    return torch.nn.functional.pad(x, flat)

kernel_name = 'sequential_preLN__ssm_conv_s4d__dense_activation_gelu__prompt'

def setup_inputs():

    _w = (lambda a: (lambda t: t.to(torch.bfloat16) if t.is_floating_point() else t)(torch.as_tensor(a)))
    def _A_setup():
        Bsz, L, D, N = 1, 2048, 448, 256
        return {
            "u": _w((lambda *s: torch.randn(*s, dtype=torch.bfloat16) * 0.05)(Bsz, L, D)),
            "log_A_real": _w((lambda *s: torch.randn(*s, dtype=torch.bfloat16) * 6.4)(D, N) * 0.5),
            "A_imag": _w((lambda *s: torch.randn(*s, dtype=torch.bfloat16) * 0.05)(D, N) * 3.14),
            "C_real": _w((lambda *s: torch.randn(*s, dtype=torch.bfloat16) * 0.05)(D, N) / N ** 0.5),
            "C_imag": _w((lambda *s: torch.randn(*s, dtype=torch.bfloat16) * 0.05)(D, N) / N ** 0.5),
            "log_dt": _w((lambda *s: torch.randn(*s, dtype=torch.bfloat16) * 0.05)(D) * 0.5 - 4.0),
            "D_skip": _w((lambda *s: torch.randn(*s, dtype=torch.bfloat16) * 0.05)(D)),
        }
    _A_in = _A_setup()

    def _B_setup():
        B, S, D, F = 1, 2048, 448, 1792
        return {
            "x": _w((lambda *s: torch.randn(*s, dtype=torch.bfloat16) * 0.05)(B, S, D)),
            "w1": _w((lambda *s: torch.randn(*s, dtype=torch.bfloat16) * 0.8)(D, F) * (D ** -0.5)),
            "b1": _w((lambda *s: torch.randn(*s, dtype=torch.bfloat16) * 0.2)(F) * 0.01),
            "w2": _w((lambda *s: torch.randn(*s, dtype=torch.bfloat16) * 0.2)(F, D) * (F ** -0.5)),
            "b2": _w((lambda *s: torch.randn(*s, dtype=torch.bfloat16) * 0.2)(D) * 0.01),
        }
    _B_in = _B_setup()

    return {
        "A_u": _A_in["u"],
        "A_log_A_real": _A_in["log_A_real"],
        "A_A_imag": _A_in["A_imag"],
        "A_C_real": _A_in["C_real"],
        "A_C_imag": _A_in["C_imag"],
        "A_log_dt": _A_in["log_dt"],
        "A_D_skip": _A_in["D_skip"],
        "B_x": _B_in["x"],
        "B_w1": _B_in["w1"],
        "B_b1": _B_in["b1"],
        "B_w2": _B_in["w2"],
        "B_b2": _B_in["b2"],
    }

def reference(A_u, A_log_A_real, A_A_imag, A_C_real, A_C_imag, A_log_dt, A_D_skip, B_x, B_w1, B_b1, B_w2, B_b2):
    def _A_ref(u, log_A_real, A_imag, C_real, C_imag, log_dt, D_skip):
        L = u.shape[1]
        dt = torch.exp(log_dt)
        A = -torch.exp(log_A_real) + 1j * A_imag
        C = (C_real + 1j * C_imag) * dt[:, None]
        pos = (lambda x, dt: x.to(dt))(torch.arange(L), log_dt.dtype)
        P = torch.exp((A * dt[:, None])[:, :, None] * pos[None, None, :])
        K = 2.0 * torch.einsum("dn,dnl->dl", C, P).real
        L2 = 2 * L
        u_t = (lambda x, *d: x.permute(*d))(u, 0, 2, 1)
        Kf = (lambda x, *a, **k: torch.fft.rfft(x.float(), *a, **k))(K, L2)
        Uf = (lambda x, *a, **k: torch.fft.rfft(x.float(), *a, **k))(u_t, L2)
        y = (lambda x, *a, **k: torch.fft.irfft(x, *a, **k).to(torch.bfloat16))(Uf * Kf[None, :, :], L2)[:, :, :L]
        y = (lambda x, *d: x.permute(*d))(y, 0, 2, 1)
        return y + u * D_skip[None, None, :]

    def _B_ref(x, w1, b1, w2, b2):
        h = torch.einsum("bsd,df->bsf", x, w1) + b1
        h = _gelu(h)
        return torch.einsum("bsf,fd->bsd", h, w2) + b2

    x = A_u
    h = x + _A_ref(_layernorm(x, axis=-1), A_log_A_real, A_A_imag, A_C_real, A_C_imag, A_log_dt, A_D_skip)
    h2 = h + _B_ref(_layernorm(h, axis=-1), B_w1, B_b1, B_w2, B_b2)
    return h2
```

```cpp
#include <hip/hip_runtime.h>
#include <hip/hip_bf16.h>
#include <stdint.h>

typedef __attribute__((ext_vector_type(8))) __bf16 bf16x8;
typedef __attribute__((ext_vector_type(4))) __bf16 bf16x4;
typedef __attribute__((ext_vector_type(2))) __bf16 bf16x2;
typedef __attribute__((ext_vector_type(8))) _Float16 f16x8;
typedef __attribute__((ext_vector_type(2))) _Float16 f16x2;
typedef __attribute__((ext_vector_type(2))) float f32x2;
typedef __attribute__((ext_vector_type(4))) float f32x4;
typedef __attribute__((ext_vector_type(16))) float f32x16;
typedef __attribute__((ext_vector_type(4))) unsigned u32x4;
typedef __attribute__((ext_vector_type(2))) unsigned u32x2;
typedef unsigned short u16;

#define DI __device__ __forceinline__

constexpr int L_ = 2048, D_ = 448, N_ = 256, F_ = 1792;
constexpr int KR_LEN = 2304;

constexpr size_t OFF_UT = 0;
constexpr size_t OFF_XT = OFF_UT + (size_t)D_ * L_ * 2;
constexpr size_t OFF_KR = OFF_XT + (size_t)D_ * L_ * 2;
constexpr size_t OFF_KS = OFF_KR + (size_t)D_ * KR_LEN * 2;
constexpr size_t OFF_CS = OFF_KS + 2048;
constexpr size_t OFF_W1 = OFF_CS + 8192;
constexpr size_t OFF_W2 = OFF_W1 + (size_t)D_ * F_ * 2;
constexpr size_t OFF_HT = OFF_W2 + (size_t)D_ * F_ * 2;
constexpr size_t OFF_H = OFF_HT + (size_t)D_ * L_ * 2;
constexpr size_t OFF_H1 = OFF_H + (size_t)D_ * L_ * 2;
constexpr size_t WS_END = OFF_H1 + (size_t)L_ * F_ * 2;

DI float bf2f(u16 v) { return __uint_as_float(((unsigned)v) << 16); }
DI float bflo(unsigned p) { return __uint_as_float(p << 16); }
DI float bfhi(unsigned p) { return __uint_as_float(p & 0xffff0000u); }
DI unsigned pack_bf16(float a, float b) {
  f32x2 v = {a, b};
  return __builtin_bit_cast(unsigned, __builtin_convertvector(v, bf16x2));
}
DI unsigned pack_f16(float a, float b) {
  f32x2 v = {a, b};
  return __builtin_bit_cast(unsigned, __builtin_convertvector(v, f16x2));
}
DI float rbf(float a) { return bflo(pack_bf16(a, a)); }
DI float wave_sum(float v) {
#pragma unroll
  for (int o = 32; o >= 1; o >>= 1) v += __shfl_xor(v, o);
  return v;
}
struct cf { float r, i; };
DI cf cmul(cf a, cf b) {
  cf o;
  o.r = __builtin_fmaf(a.r, b.r, -(a.i * b.i));
  o.i = __builtin_fmaf(a.r, b.i, a.i * b.r);
  return o;
}
DI cf cexp_p(float ar2, float air, float p) {
  float mag = __builtin_amdgcn_exp2f(ar2 * p);
  float ph = __builtin_amdgcn_fractf(air * p);
  cf o;
  o.r = mag * __builtin_amdgcn_cosf(ph);
  o.i = mag * __builtin_amdgcn_sinf(ph);
  return o;
}
DI int perm23(int r) { return (r & ~12) | ((r & 4) << 1) | ((r & 8) >> 1); }

constexpr int K1_KGEN = 448, K1_LN = 64, K1_W1 = 56, K1_W2 = 56;
constexpr int K1_GRID = K1_KGEN + K1_LN + K1_W1 + K1_W2;
constexpr int KG_RS = 272;
constexpr int KG_ROWS = 16 + 48;
constexpr int KG_WAVE = (16 + 41) * KG_RS;
constexpr int KG_PART = 4 * KG_WAVE + 7 * KG_RS + 64;
constexpr int K1_LDS = KG_PART + 4 * 48 * 16 * 4 + 64;

DI void k1_kgen(char* smem, int d, const u16* __restrict__ lar_p, const u16* __restrict__ aim_p,
                const u16* __restrict__ cr_p, const u16* __restrict__ ci_p, const u16* __restrict__ ldt_p,
                char* __restrict__ ws) {
  const int tid = threadIdx.x, lane = tid & 63, wv = tid >> 6;
  const int idx = d * N_ + tid;
  const float lar = bf2f(lar_p[idx]), aim = bf2f(aim_p[idx]), cr = bf2f(cr_p[idx]), ci = bf2f(ci_p[idx]);
  const float ldt = bf2f(ldt_p[d]);
  const float dt = rbf(expf(ldt));
  const float are = -rbf(expf(lar));
  const float ar = are * dt, ai = aim * dt;
  const float Cr = cr * dt, Ci = ci * dt;
  float* red = (float*)(smem + K1_LDS - 64);
  float s = wave_sum(2.f * (fabsf(Cr) + fabsf(Ci)));
  if (lane == 0) red[wv] = s;
  __syncthreads();
  s = red[0] + red[1] + red[2] + red[3];
  int ex = (s > 0.f) ? __builtin_amdgcn_frexp_expf(s) : 14;
  ex = max(ex, -100);
  ex = min(ex, 120);
  const float scale = ldexpf(1.f, 14 - ex), inv_scale = ldexpf(1.f, ex - 14);

  const float ar2 = ar * 1.4426950408889634f, air = ai * 0.15915494309189535f;
  const cf E1 = cexp_p(ar2, air, 1.f), E8 = cexp_p(ar2, air, 8.f), E128 = cexp_p(ar2, air, 128.f);

  char* wb = smem + wv * KG_WAVE + lane * 4;
  {
    cf l = {1.f, 0.f};
#pragma unroll
    for (int k = 0; k < 16; ++k) {
      *(unsigned*)(wb + k * KG_RS) = pack_f16(l.r, -l.i);
      if (k < 15) l = (k == 0) ? E8 : cmul(l, E8);
    }
  }
  char* hb = wb + 16 * KG_RS;
  cf g[8];
  g[0].r = 2.f * scale * Cr; g[0].i = 2.f * scale * Ci;
#pragma unroll
  for (int c = 1; c < 8; ++c) g[c] = cmul(g[c - 1], E1);
#pragma unroll
  for (int c = 0; c < 8; ++c) *(unsigned*)(hb + c * KG_RS) = pack_f16(g[c].r, g[c].i);
  cf R = E128;
#pragma unroll
  for (int c = 0; c < 8; ++c) { cf v = cmul(g[c], R); *(unsigned*)(hb + (8 + c) * KG_RS) = pack_f16(v.r, v.i); }
#pragma unroll
  for (int h = 0; h < 2; ++h) {
    R = cmul(R, E128);
#pragma unroll
    for (int c = 0; c < 4; ++c) { cf v = cmul(g[2 * c], R); *(unsigned*)(hb + (16 + 4 * h + c) * KG_RS) = pack_f16(v.r, v.i); }
  }
#pragma unroll
  for (int h = 0; h < 4; ++h) {
    R = cmul(R, E128);
#pragma unroll
    for (int c = 0; c < 2; ++c) { cf v = cmul(g[4 * c], R); *(unsigned*)(hb + (24 + 2 * h + c) * KG_RS) = pack_f16(v.r, v.i); }
  }
  R = cmul(R, E128);
  {
    cf z = cmul(g[0], R);
#pragma unroll
    for (int h = 0; h < 9; ++h) {
      *(unsigned*)(hb + (32 + h) * KG_RS) = pack_f16(z.r, z.i);
      if (h < 8) z = cmul(z, E128);
    }
  }
  __syncthreads();
  {
    const int r = lane & 15, gq = lane >> 4;
    const char* base = smem + wv * KG_WAVE + r * KG_RS + gq * 16;
    f32x4 acc[3] = {};
#pragma unroll
    for (int ks = 0; ks < 4; ++ks) {
      const f16x8 b = *(const f16x8*)(base + ks * 64);
#pragma unroll
      for (int m = 0; m < 3; ++m) {
        const f16x8 a = *(const f16x8*)(base + (16 + 16 * m) * KG_RS + ks * 64);
        acc[m] = __builtin_amdgcn_mfma_f32_16x16x32_f16(a, b, acc[m], 0, 0, 0);
      }
    }
    float* part = (float*)(smem + KG_PART) + wv * 768;
#pragma unroll
    for (int m = 0; m < 3; ++m)
#pragma unroll
      for (int j = 0; j < 4; ++j) part[(16 * m + 4 * gq + j) * 16 + r] = acc[m][j];
  }
  __syncthreads();
  {
    const float* part = (const float*)(smem + KG_PART);
    float v[8];
#pragma unroll
    for (int j = 0; j < 8; ++j) {
      const int l = 2047 - (tid * 8 + j);
      unsigned b = __float_as_uint((float)l);
      b = (b + 0x7fffu + ((b >> 16) & 1u)) & 0xffff0000u;
      const int p = (int)__uint_as_float(b);
      int e = 24 - __clz(p | 1);
      e = min(max(e, 0), 3);
      const int row = 8 * e + ((p >> 7) << (3 - e)) + ((p & 7) >> e);
      const int col = (p & 127) >> 3;
      const int o = row * 16 + col;
      v[j] = (part[o] + part[768 + o]) + (part[1536 + o] + part[2304 + o]);
    }
    u32x4 pk = {pack_f16(v[0], v[1]), pack_f16(v[2], v[3]), pack_f16(v[4], v[5]), pack_f16(v[6], v[7])};
    char* kr = ws + OFF_KR + (size_t)d * KR_LEN * 2;
    *(u32x4*)(kr + tid * 16) = pk;
    if (tid < 32) *(u32x4*)(kr + 4096 + tid * 16) = (u32x4){0u, 0u, 0u, 0u};
    if (tid == 0) ((float*)(ws + OFF_KS))[d] = inv_scale;
  }
}

constexpr int LN_VS = 448 * 16 + 56 * 16 + 64;
DI void k1_ln(char* smem, int blk, const u16* __restrict__ x, char* __restrict__ ws) {
  const int tid = threadIdx.x, lane = tid & 63, wv = tid >> 6;
  const int tok0 = blk * 32 + wv * 8;
  const bool act = lane < 56;
  u32x4 xr[8];
#pragma unroll
  for (int t = 0; t < 8; ++t) {
    xr[t] = (u32x4){0u, 0u, 0u, 0u};
    if (act) xr[t] = *(const u32x4*)(x + (size_t)(tok0 + t) * D_ + lane * 8);
  }
  float xf[8][8];
  float mean[8], rs[8], mb[8];
#pragma unroll
  for (int t = 0; t < 8; ++t) {
    float s = 0.f;
#pragma unroll
    for (int j = 0; j < 4; ++j) {
      xf[t][2 * j] = bflo(xr[t][j]);
      xf[t][2 * j + 1] = bfhi(xr[t][j]);
      s += xf[t][2 * j] + xf[t][2 * j + 1];
    }
    mean[t] = s;
  }
#pragma unroll
  for (int t = 0; t < 8; ++t) mean[t] = wave_sum(mean[t]) * (1.f / D_);
#pragma unroll
  for (int t = 0; t < 8; ++t) {
    float q = 0.f;
#pragma unroll
    for (int j = 0; j < 8; ++j) { float dd = xf[t][j] - mean[t]; q = __builtin_fmaf(dd, dd, q); }
    rs[t] = act ? q : 0.f;
  }
#pragma unroll
  for (int t = 0; t < 8; ++t) {
    float var = wave_sum(rs[t]) * (1.f / D_);
    float vb = rbf(var);
    float sd = rbf(sqrtf(rbf(vb + 1e-5f)));
    rs[t] = sd;
    mb[t] = rbf(mean[t]);
  }
  char* ub = smem + wv * LN_VS;
  char* xb = smem + 4 * LN_VS + wv * LN_VS;
  if (act) {
#pragma unroll
    for (int j = 0; j < 8; ++j) {
      float un[8];
#pragma unroll
      for (int t = 0; t < 8; ++t) un[t] = rbf(xf[t][j] - mb[t]) / rs[t];
      u32x4 pu = {pack_bf16(un[0], un[1]), pack_bf16(un[2], un[3]), pack_bf16(un[4], un[5]), pack_bf16(un[6], un[7])};
      u32x4 px;
#pragma unroll
      for (int t = 0; t < 4; ++t) {
        const unsigned a = xr[2 * t][j >> 1], b = xr[2 * t + 1][j >> 1];
        px[t] = (j & 1) ? ((a >> 16) | (b & 0xffff0000u)) : ((a & 0xffffu) | (b << 16));
      }
      const int ch = lane * 8 + j;
      *(u32x4*)(ub + ch * 16 + lane * 16) = pu;
      *(u32x4*)(xb + ch * 16 + lane * 16) = px;
    }
  }
  __syncthreads();
  u16* uT = (u16*)(ws + OFF_UT);
  u16* xT = (u16*)(ws + OFF_XT);
#pragma unroll
  for (int i = 0; i < 7; ++i) {
    const int ch = i * 64 + (tid >> 2), v = tid & 3;
    const int o = v * LN_VS + ch * 16 + (ch >> 3) * 16;
    const u32x4 a = *(const u32x4*)(smem + o);
    const u32x4 b = *(const u32x4*)(smem + 4 * LN_VS + o);
    *(u32x4*)(uT + (size_t)ch * L_ + blk * 32 + v * 8) = a;
    *(u32x4*)(xT + (size_t)ch * L_ + blk * 32 + v * 8) = b;
  }
}

DI void k1_wtile(char* smem, const u16* __restrict__ src, int ldn, u16* __restrict__ dst, float* __restrict__ cs) {
  const int tid = threadIdx.x;
  u16* t = (u16*)smem;
#pragma unroll
  for (int i = 0; i < 7; ++i) {
    const int c = i * 256 + tid, k = c >> 2, q = c & 3;
    const u32x4 v = *(const u32x4*)(src + (size_t)k * ldn + q * 8);
    *(u32x2*)(t + k * 36 + q * 8) = (u32x2){v[0], v[1]};
    *(u32x2*)(t + k * 36 + q * 8 + 4) = (u32x2){v[2], v[3]};
  }
  __syncthreads();
#pragma unroll
  for (int i = 0; i < 7; ++i) {
    const int c = i * 256 + tid, ks = c >> 6, l = c & 63;
    const int col = perm23(l & 31), k0 = ks * 16 + (l >> 5) * 8;
    unsigned w[4];
#pragma unroll
    for (int j = 0; j < 4; ++j)
      w[j] = (unsigned)t[(k0 + 2 * j) * 36 + col] | ((unsigned)t[(k0 + 2 * j + 1) * 36 + col] << 16);
    *(u32x4*)(dst + (size_t)c * 8) = (u32x4){w[0], w[1], w[2], w[3]};
  }
  if (cs) {
    const int col = tid & 31, kg = tid >> 5;
    float s = 0.f;
    for (int k = kg * 56; k < kg * 56 + 56; ++k) s += bf2f(t[k * 36 + col]);
    float* red = (float*)(smem + 448 * 72);
    red[tid] = s;
    __syncthreads();
    if (tid < 32) {
      float a = 0.f;
#pragma unroll
      for (int g = 0; g < 8; ++g) a += red[g * 32 + tid];
      cs[tid] = a;
    }
  }
}

__global__ __launch_bounds__(256) void k1_kernel(const u16* __restrict__ x, const u16* __restrict__ lar,
                                                 const u16* __restrict__ aim, const u16* __restrict__ cr,
                                                 const u16* __restrict__ ci, const u16* __restrict__ ldt,
                                                 const u16* __restrict__ w1, const u16* __restrict__ w2,
                                                 char* __restrict__ ws) {
  __shared__ __attribute__((aligned(16))) char smem[K1_LDS];
  int b = blockIdx.x;
  if (b < K1_LN) { k1_ln(smem, b, x, ws); return; }
  b -= K1_LN;
  if (b < K1_KGEN) { k1_kgen(smem, b, lar, aim, cr, ci, ldt, ws); return; }
  b -= K1_KGEN;
  if (b < K1_W1) {
    k1_wtile(smem, w1 + b * 32, F_, (u16*)(ws + OFF_W1) + (size_t)b * 28 * 512, (float*)(ws + OFF_CS) + b * 32);
    return;
  }
  b -= K1_W1;
  {
    const int dt = b >> 2, kc = b & 3;
    k1_wtile(smem, w2 + (size_t)kc * 448 * D_ + dt * 32, D_, (u16*)(ws + OFF_W2) + ((size_t)dt * 112 + kc * 28) * 512,
             nullptr);
  }
}

constexpr int CV_CS = 4160;
constexpr int CV_KOFF = 8 * CV_CS;
constexpr int CV_LDS = CV_KOFF + KR_LEN * 2;

__global__ __launch_bounds__(256) void k2_kernel(const u16* __restrict__ dskip, char* __restrict__ ws) {
  __shared__ __attribute__((aligned(16))) char smem[CV_LDS];
  const int d = blockIdx.x, tid = threadIdx.x, lane = tid & 63, wv = tid >> 6;
  const u16* uT = (const u16*)(ws + OFF_UT) + (size_t)d * L_;
  const u16* xT = (const u16*)(ws + OFF_XT) + (size_t)d * L_;
  const char* kr = ws + OFF_KR + (size_t)d * KR_LEN * 2;
  u32x4 c0 = {0u, 0u, 0u, 0u};
  if (tid > 0) c0 = *(const u32x4*)(uT + (tid - 1) * 8);
  const u32x4 c1 = *(const u32x4*)(uT + tid * 8);
  const u32x4 k0 = *(const u32x4*)(kr + tid * 16);
  u32x4 k1 = {0u, 0u, 0u, 0u};
  if (tid < 32) k1 = *(const u32x4*)(kr + 4096 + tid * 16);
  const int c16 = lane & 15, g4 = lane >> 4;
  u32x2 ue[2], xe[2];
#pragma unroll
  for (int i = 0; i < 2; ++i) {
    const int p = 256 * (2 * wv + i) + 16 * c16 + 4 * g4;
    ue[i] = *(const u32x2*)(uT + p);
    xe[i] = *(const u32x2*)(xT + p);
  }
  const float inv_scale = ((const float*)(ws + OFF_KS))[d];
  const float dsk = bf2f(dskip[d]);
  *(u32x4*)(smem + CV_KOFF + tid * 16) = k0;
  if (tid < 32) *(u32x4*)(smem + CV_KOFF + 4096 + tid * 16) = k1;
  if (tid < 8) *(u32x4*)(smem + tid * CV_CS) = (u32x4){0u, 0u, 0u, 0u};
  {
    unsigned p[8];
#pragma unroll
    for (int j = 0; j < 4; ++j) {
      p[j] = pack_f16(bflo(c0[j]), bfhi(c0[j]));
      p[4 + j] = pack_f16(bflo(c1[j]), bfhi(c1[j]));
    }
    unsigned q[7];
#pragma unroll
    for (int i = 0; i < 7; ++i) q[i] = __builtin_amdgcn_alignbit(p[i + 1], p[i], 16);
    char* dst = smem + (tid + 1) * 16;
#pragma unroll
    for (int rho = 0; rho < 8; ++rho) {
      const int off = rho + 1;
      u32x4 w;
      if (off & 1) { const int k = off >> 1; w = (u32x4){q[k], q[k + 1], q[k + 2], q[k + 3]}; }
      else { const int k = off >> 1; w = (u32x4){p[k], p[k + 1], p[k + 2], p[k + 3]}; }
      *(u32x4*)(dst + rho * CV_CS) = w;
    }
  }
  __syncthreads();
  f32x4 acc[8] = {};
  const char* abase = smem + (c16 & 7) * CV_CS + ((c16 & 8) + 8 * g4) * 2;
  const char* bbase = smem + CV_KOFF + (2032 - 16 * c16 + 8 * g4) * 2;
#pragma unroll
  for (int si = 0; si < 2; ++si) {
    const int sg = 2 * wv + si;
    f16x8 A[8], B[8];
#pragma unroll
    for (int i = 0; i < 8; ++i) {
      A[i] = *(const f16x8*)(abase + sg * 64 + i * 512);
      B[i] = *(const f16x8*)(bbase + sg * 64 - i * 512);
    }
#pragma unroll
    for (int ia = 0; ia < 8; ++ia)
#pragma unroll
      for (int ib = 0; ib + ia < 8; ++ib)
        acc[ia + ib] = __builtin_amdgcn_mfma_f32_16x16x32_f16(A[ia], B[ib], acc[ia + ib], 0, 0, 0);
  }
  __syncthreads();
  f32x4* red = (f32x4*)smem;
#pragma unroll
  for (int t = 0; t < 8; ++t) red[(t * 4 + wv) * 64 + lane] = acc[t];
  __syncthreads();
  u16* hT = (u16*)(ws + OFF_HT) + (size_t)d * L_;
#pragma unroll
  for (int i = 0; i < 2; ++i) {
    const int t = 2 * wv + i;
    const f32x4 s = (red[(t * 4 + 0) * 64 + lane] + red[(t * 4 + 1) * 64 + lane]) +
                    (red[(t * 4 + 2) * 64 + lane] + red[(t * 4 + 3) * 64 + lane]);
    float h[4];
#pragma unroll
    for (int j = 0; j < 4; ++j) {
      const float uu = (j & 1) ? bfhi(ue[i][j >> 1]) : bflo(ue[i][j >> 1]);
      const float xx = (j & 1) ? bfhi(xe[i][j >> 1]) : bflo(xe[i][j >> 1]);
      const float y = rbf(s[j] * inv_scale);
      const float tt = rbf(y + rbf(uu * dsk));
      h[j] = xx + tt;
    }
    *(u32x2*)(hT + 256 * t + 16 * c16 + 4 * g4) = (u32x2){pack_bf16(h[0], h[1]), pack_bf16(h[2], h[3])};
  }
}

constexpr int K3_TILE = 448 * 256;
constexpr int K3_LDS = K3_TILE + 128 * 8;
DI unsigned swz_b(unsigned row) { return ((row & 3) << 2) | ((row >> 2) & 3); }
typedef __attribute__((address_space(3))) bf16x4* lds_bf16x4_p;

DI bf16x8 k3_bfrag(const char* smem, unsigned lane_off0, unsigned lane_off1, int ks) {
  const bf16x4 lo = __builtin_amdgcn_ds_read_tr16_b64_v4bf16((lds_bf16x4_p)(smem + lane_off0 + ks * 4096));
  const bf16x4 hi = __builtin_amdgcn_ds_read_tr16_b64_v4bf16((lds_bf16x4_p)(smem + lane_off1 + ks * 4096));
  return __builtin_shufflevector(lo, hi, 0, 1, 2, 3, 4, 5, 6, 7);
}

__global__ __launch_bounds__(256, 1) void k3_kernel(const u16* __restrict__ b1, char* __restrict__ ws) {
  __shared__ __attribute__((aligned(16))) char smem[K3_LDS];
  const int tid = threadIdx.x, lane = tid & 63;
  const int wv = __builtin_amdgcn_readfirstlane(tid >> 6);
  const int xcd = blockIdx.x & 7, idx = blockIdx.x >> 3;
  const int tt = (xcd >> 1) * 4 + idx / 7, fs = (xcd & 1) * 7 + idx % 7;
  const int tok0 = tt * 128, ft = fs * 4 + wv;
  const u16* hT = (const u16*)(ws + OFF_HT);
#pragma unroll
  for (int i = 0; i < 28; ++i) {
    const unsigned row = (i * 4 + wv) * 4 + (lane >> 4), p = lane & 15;
    const unsigned chk = p ^ swz_b(row);
    __builtin_amdgcn_global_load_lds((const unsigned*)(hT + (size_t)row * L_ + tok0 + chk * 8),
                                     (__attribute__((address_space(3))) unsigned*)(smem + (i * 4 + wv) * 1024), 16, 0, 0);
  }
  const bf16x8* w1f = (const bf16x8*)(ws + OFF_W1) + (size_t)ft * 28 * 64 + lane;
  bf16x8 A[28];
#pragma unroll
  for (int ks = 0; ks < 28; ++ks) A[ks] = w1f[ks * 64];
  const int hh = lane >> 5, col = lane & 31;
  float cs[16], bb[16];
  {
    const float* csg = (const float*)(ws + OFF_CS) + ft * 32 + 8 * hh;
    const u16* b1g = b1 + ft * 32 + 8 * hh;
#pragma unroll
    for (int s = 0; s < 2; ++s) {
      const f32x4 c0 = *(const f32x4*)(csg + 16 * s), c1 = *(const f32x4*)(csg + 16 * s + 4);
      const u32x4 bv = *(const u32x4*)(b1g + 16 * s);
#pragma unroll
      for (int j = 0; j < 4; ++j) {
        cs[8 * s + j] = c0[j];
        cs[8 * s + 4 + j] = c1[j];
        bb[8 * s + 2 * j] = bflo(bv[j]);
        bb[8 * s + 2 * j + 1] = bfhi(bv[j]);
      }
    }
  }
  __syncthreads();
  const unsigned q = (lane & 15) >> 2, p = lane & 3, blk = (lane >> 4) & 1;
  float2* stats = (float2*)(smem + K3_TILE);
  bf16x8 ones;
#pragma unroll
  for (int j = 0; j < 8; ++j) ones[j] = (__bf16)1.0f;
  bf16x8* h1f = (bf16x8*)(ws + OFF_H1);
#pragma unroll
  for (int it = 0; it < 4; ++it) {
    const unsigned c = (wv + it) & 3;
    const unsigned r0 = 8 * hh + q, r1 = 8 * hh + 4 + q;
    const unsigned chk = 4 * c + 2 * blk + (p >> 1);
    const unsigned o0 = 256 * r0 + 16 * (chk ^ swz_b(r0)) + 8 * (p & 1);
    const unsigned o1 = 256 * r1 + 16 * (chk ^ swz_b(r1)) + 8 * (p & 1);
    f32x16 acc = {};
    if (it == 0) {
      f32x16 accS = {}, accQ = {};
#pragma unroll
      for (int ks = 0; ks < 28; ++ks) {
        const bf16x8 B = k3_bfrag(smem, o0, o1, ks);
        acc = __builtin_amdgcn_mfma_f32_32x32x16_bf16(A[ks], B, acc, 0, 0, 0);
        accS = __builtin_amdgcn_mfma_f32_32x32x16_bf16(ones, B, accS, 0, 0, 0);
        accQ = __builtin_amdgcn_mfma_f32_32x32x16_bf16(B, B, accQ, 0, 0, 0);
      }
      const int myreg = (col & 3) + 4 * (col >> 3);
      float qd = 0.f;
#pragma unroll
      for (int r = 0; r < 16; ++r) qd = (r == myreg) ? accQ[r] : qd;
      const float mean = accS[0] * (1.f / D_);
      const float var = fmaxf(__builtin_fmaf(-mean, mean, qd * (1.f / D_)), 0.f);
      const float sd = rbf(sqrtf(rbf(rbf(var) + 1e-5f)));
      if (hh == ((col >> 2) & 1)) stats[c * 32 + col] = make_float2(rbf(mean), 1.f / sd);
      u16* hg = (u16*)(ws + OFF_H) + (size_t)(tok0 + c * 32 + col) * D_ + 8 * hh;
#pragma unroll
      for (int j = 0; j < 2; ++j) {
        const int ks = 2 * fs + j;
        const bf16x8 B = k3_bfrag(smem, o0, o1, ks);
        *(bf16x8*)(hg + 16 * ks) = B;
      }
      __syncthreads();
    } else {
#pragma unroll
      for (int ks = 0; ks < 28; ++ks) {
        const bf16x8 B = k3_bfrag(smem, o0, o1, ks);
        acc = __builtin_amdgcn_mfma_f32_32x32x16_bf16(A[ks], B, acc, 0, 0, 0);
      }
    }
    const float2 st = stats[c * 32 + col];
    unsigned pk[8];
#pragma unroll
    for (int r = 0; r < 16; r += 2) {
      float gv[2];
#pragma unroll
      for (int e = 0; e < 2; ++e) {
        const float t1 = __builtin_fmaf(-st.x, cs[r + e], acc[r + e]);
        const float v = __builtin_fmaf(t1, st.y, bb[r + e]);
        const float w = v * v;
        const float z = v * __builtin_fmaf(w, -0.10294324f, -2.3022082f);
        const float ex = __builtin_amdgcn_exp2f(z);
        gv[e] = v * __builtin_amdgcn_rcpf(1.f + ex);
      }
      pk[r >> 1] = pack_bf16(gv[0], gv[1]);
    }
    const size_t fb = ((size_t)(tt * 4 + c) * 112 + 2 * ft) * 64 + lane;
    *(u32x4*)(h1f + fb) = (u32x4){pk[0], pk[1], pk[2], pk[3]};
    *(u32x4*)(h1f + fb + 64) = (u32x4){pk[4], pk[5], pk[6], pk[7]};
  }
}

constexpr int K4_DEPTH = 7;
__global__ __launch_bounds__(256, 1) void k4_kernel(const u16* __restrict__ b2, const char* __restrict__ ws,
                                                    u16* __restrict__ out) {
  __shared__ __attribute__((aligned(16))) f32x4 red[4 * 4 * 4 * 64];
  const int tid = threadIdx.x, lane = tid & 63;
  const int wv = __builtin_amdgcn_readfirstlane(tid >> 6);
  const int xcd = blockIdx.x & 7, idx = blockIdx.x >> 3;
  const int tt = xcd * 4 + idx / 7, dj = idx % 7;
  const bf16x8* wa = (const bf16x8*)(ws + OFF_W2) + ((size_t)(2 * dj) * 112 + wv * 28) * 64 + lane;
  const bf16x8* hb = (const bf16x8*)(ws + OFF_H1) + ((size_t)(2 * tt) * 112 + wv * 28) * 64 + lane;
  bf16x8 a0[K4_DEPTH], a1[K4_DEPTH], b0[K4_DEPTH], b1v[K4_DEPTH];
#pragma unroll
  for (int i = 0; i < K4_DEPTH; ++i) {
    a0[i] = wa[i * 64]; a1[i] = wa[(112 + i) * 64];
    b0[i] = hb[i * 64]; b1v[i] = hb[(112 + i) * 64];
  }
  const int hh = lane >> 5, col = lane & 31;
  const int tok = tt * 64 + (wv & 1) * 32 + col, d0 = dj * 64 + (wv >> 1) * 32 + 8 * hh;
  const u16* hrow = (const u16*)(ws + OFF_H) + (size_t)tok * D_ + d0;
  const u32x4 hr0 = *(const u32x4*)(hrow), hr1 = *(const u32x4*)(hrow + 16);
  const u32x4 bz0 = *(const u32x4*)(b2 + d0), bz1 = *(const u32x4*)(b2 + d0 + 16);
  f32x16 acc[4] = {};
#pragma unroll
  for (int ks = 0; ks < 28; ++ks) {
    const int sl = ks % K4_DEPTH;
    const bf16x8 x0 = a0[sl], x1 = a1[sl], y0 = b0[sl], y1 = b1v[sl];
    if (ks + K4_DEPTH < 28) {
      a0[sl] = wa[(ks + K4_DEPTH) * 64]; a1[sl] = wa[(112 + ks + K4_DEPTH) * 64];
      b0[sl] = hb[(ks + K4_DEPTH) * 64]; b1v[sl] = hb[(112 + ks + K4_DEPTH) * 64];
    }
    acc[0] = __builtin_amdgcn_mfma_f32_32x32x16_bf16(x0, y0, acc[0], 0, 0, 0);
    acc[1] = __builtin_amdgcn_mfma_f32_32x32x16_bf16(x0, y1, acc[1], 0, 0, 0);
    acc[2] = __builtin_amdgcn_mfma_f32_32x32x16_bf16(x1, y0, acc[2], 0, 0, 0);
    acc[3] = __builtin_amdgcn_mfma_f32_32x32x16_bf16(x1, y1, acc[3], 0, 0, 0);
  }
#pragma unroll
  for (int t = 0; t < 4; ++t)
#pragma unroll
    for (int g = 0; g < 4; ++g)
      red[((t * 4 + wv) * 4 + g) * 64 + lane] = (f32x4){acc[t][4 * g], acc[t][4 * g + 1], acc[t][4 * g + 2], acc[t][4 * g + 3]};
  __syncthreads();
  float o[16];
#pragma unroll
  for (int g = 0; g < 4; ++g) {
    const f32x4 s = (red[((wv * 4 + 0) * 4 + g) * 64 + lane] + red[((wv * 4 + 1) * 4 + g) * 64 + lane]) +
                    (red[((wv * 4 + 2) * 4 + g) * 64 + lane] + red[((wv * 4 + 3) * 4 + g) * 64 + lane]);
#pragma unroll
    for (int j = 0; j < 4; ++j) o[4 * g + j] = s[j];
  }
  unsigned pk[8];
#pragma unroll
  for (int r = 0; r < 16; r += 2) {
    const unsigned hv = (r < 8) ? hr0[r >> 1] : hr1[(r - 8) >> 1];
    const unsigned bv = (r < 8) ? bz0[r >> 1] : bz1[(r - 8) >> 1];
    const float e0 = bflo(hv) + rbf(rbf(o[r]) + bflo(bv));
    const float e1 = bfhi(hv) + rbf(rbf(o[r + 1]) + bfhi(bv));
    pk[r >> 1] = pack_bf16(e0, e1);
  }
  u16* orow = out + (size_t)tok * D_ + d0;
  *(u32x4*)(orow) = (u32x4){pk[0], pk[1], pk[2], pk[3]};
  *(u32x4*)(orow + 16) = (u32x4){pk[4], pk[5], pk[6], pk[7]};
}

extern "C" void kernel_launch(void* const* d_in, const int* in_sizes, int n_in, void* d_out, int out_size, void* d_ws,
                              size_t ws_size, hipStream_t stream) {
  const u16* x = (const u16*)d_in[0];
  const u16* lar = (const u16*)d_in[1];
  const u16* aim = (const u16*)d_in[2];
  const u16* cr = (const u16*)d_in[3];
  const u16* ci = (const u16*)d_in[4];
  const u16* ldt = (const u16*)d_in[5];
  const u16* dsk = (const u16*)d_in[6];
  const u16* w1 = (const u16*)d_in[8];
  const u16* b1 = (const u16*)d_in[9];
  const u16* w2 = (const u16*)d_in[10];
  const u16* b2 = (const u16*)d_in[11];
  char* ws = (char*)d_ws;
  k1_kernel<<<K1_GRID, 256, 0, stream>>>(x, lar, aim, cr, ci, ldt, w1, w2, ws);
  k2_kernel<<<D_, 256, 0, stream>>>(dsk, ws);
  k3_kernel<<<224, 256, 0, stream>>>(b1, ws);
  k4_kernel<<<224, 256, 0, stream>>>(b2, ws, (u16*)d_out);
}
```

```cpp
#include <hip/hip_runtime.h>
#include <hip/hip_bf16.h>
#include <stdint.h>

typedef __attribute__((ext_vector_type(8))) __bf16 bf16x8;
typedef __attribute__((ext_vector_type(4))) __bf16 bf16x4;
typedef __attribute__((ext_vector_type(2))) __bf16 bf16x2;
typedef __attribute__((ext_vector_type(8))) _Float16 f16x8;
typedef __attribute__((ext_vector_type(2))) _Float16 f16x2;
typedef __attribute__((ext_vector_type(2))) float f32x2;
typedef __attribute__((ext_vector_type(4))) float f32x4;
typedef __attribute__((ext_vector_type(16))) float f32x16;
typedef __attribute__((ext_vector_type(4))) unsigned u32x4;
typedef __attribute__((ext_vector_type(2))) unsigned u32x2;
typedef unsigned short u16;

#define DI __device__ __forceinline__
#define SCHED_FENCE() __builtin_amdgcn_sched_barrier(0)
#define STAMP_BASE(ws, kid)
#define STAMP(i)
#define STAMPW(i)

constexpr int L_ = 2048, D_ = 448, N_ = 256, F_ = 1792;
constexpr int KR_LEN = 2304;

constexpr size_t OFF_UT = 0;
constexpr size_t OFF_XT = OFF_UT + (size_t)D_ * L_ * 2;
constexpr size_t OFF_KR = OFF_XT + (size_t)D_ * L_ * 2;
constexpr size_t OFF_KS = OFF_KR + (size_t)D_ * KR_LEN * 2;
constexpr int W1_FRAGS = 29;
constexpr size_t OFF_CNT = OFF_KS + 2048;
constexpr size_t OFF_W1 = OFF_CNT + 8192;
constexpr size_t OFF_W2 = OFF_W1 + (size_t)56 * W1_FRAGS * 1024;
constexpr int HT_LD = L_ + 128;
constexpr size_t OFF_HT = OFF_W2 + (size_t)D_ * F_ * 2;
constexpr size_t OFF_H1 = OFF_HT + (size_t)D_ * HT_LD * 2;
constexpr size_t WS_END = OFF_H1 + (size_t)L_ * F_ * 2;

DI float bf2f(u16 v) { return __uint_as_float(((unsigned)v) << 16); }
DI float bflo(unsigned p) { return __uint_as_float(p << 16); }
DI float bfhi(unsigned p) { return __uint_as_float(p & 0xffff0000u); }
DI unsigned pack_bf16(float a, float b) {
  f32x2 v = {a, b};
  return __builtin_bit_cast(unsigned, __builtin_convertvector(v, bf16x2));
}
DI unsigned pack_f16(float a, float b) {
  f32x2 v = {a, b};
  return __builtin_bit_cast(unsigned, __builtin_convertvector(v, f16x2));
}
DI float rbf(float a) { return bflo(pack_bf16(a, a)); }
template <int CTRL> DI float dpp_add(float v) {
  const int t = __builtin_amdgcn_update_dpp(0, __float_as_int(v), CTRL, 0xf, 0xf, true);
  return v + __int_as_float(t);
}
DI float wave_sum(float v) {
  v = dpp_add<0xB1>(v);
  v = dpp_add<0x4E>(v);
  v = dpp_add<0x141>(v);
  v = dpp_add<0x140>(v);
  auto r = __builtin_amdgcn_permlane16_swap(__float_as_uint(v), __float_as_uint(v), false, false);
  v = __uint_as_float(r[0]) + __uint_as_float(r[1]);
  auto r2 = __builtin_amdgcn_permlane32_swap(__float_as_uint(v), __float_as_uint(v), false, false);
  return __uint_as_float(r2[0]) + __uint_as_float(r2[1]);
}
#define WT_AUX 16
DI __amdgpu_buffer_rsrc_t ws_rsrc(char* ws) { return __builtin_amdgcn_make_buffer_rsrc((void*)ws, 0, (int)WS_END, 0x00020000); }
DI void wst128(__amdgpu_buffer_rsrc_t rs, size_t off, u32x4 v) { __builtin_amdgcn_raw_buffer_store_b128(v, rs, (unsigned)off, 0, WT_AUX); }
DI void wst64(__amdgpu_buffer_rsrc_t rs, size_t off, u32x2 v) { __builtin_amdgcn_raw_buffer_store_b64(v, rs, (unsigned)off, 0, WT_AUX); }
DI f32x2 cswz(f32x2 b) { return (f32x2){-b.y, b.x}; }
DI f32x2 cmul(f32x2 a, f32x2 b, f32x2 bs) {
  const f32x2 t = (f32x2){a.x, a.x} * b;
  return __builtin_elementwise_fma((f32x2){a.y, a.y}, bs, t);
}
DI f32x2 cexp_p(float ar2, float air, float p) {
  const float mag = __builtin_amdgcn_exp2f(ar2 * p);
  const float ph = __builtin_amdgcn_fractf(air * p);
  return (f32x2){mag * __builtin_amdgcn_cosf(ph), mag * __builtin_amdgcn_sinf(ph)};
}
DI unsigned pk16(f32x2 v) { return __builtin_bit_cast(unsigned, __builtin_convertvector(v, f16x2)); }
DI void pow2_scale(float s, float& scale, float& inv) {
  int ex = (s > 0.f) ? __builtin_amdgcn_frexp_expf(s) : 14;
  ex = min(max(ex, -100), 120);
  scale = ldexpf(1.f, 14 - ex);
  inv = ldexpf(1.f, ex - 14);
}
DI int perm23(int r) { return (r & ~12) | ((r & 4) << 1) | ((r & 8) >> 1); }

constexpr int K1_KGEN = 448, K1_LN = 64;
constexpr int K1_GRID = K1_KGEN + K1_LN;
constexpr int KG_RS = 272;
constexpr int KG_ROWS = 16 + 48;
constexpr int KG_WAVE = (16 + 41) * KG_RS;
constexpr int KG_PART = 4 * KG_WAVE + 7 * KG_RS + 64;
constexpr int K1_LDS = KG_PART + 4 * 48 * 16 * 4 + 64;

struct KTab { unsigned short v[2048]; };
constexpr KTab make_ktab() {
  KTab t{};
  for (int i = 0; i < 2048; ++i) {
    const int l = 2047 - i;
    int e = 0;
    while ((l >> (8 + e)) != 0) ++e;
    int p = l;
    if (e > 0) {
      const int q = l >> e, rem = l & ((1 << e) - 1), half = 1 << (e - 1);
      p = (q + ((rem > half || (rem == half && (q & 1))) ? 1 : 0)) << e;
    }
    int e2 = 0;
    while ((p >> (8 + e2)) != 0) ++e2;
    if (e2 > 3) e2 = 3;
    const int row = 8 * e2 + ((p >> 7) << (3 - e2)) + ((p & 7) >> e2);
    const int col = (p & 127) >> 3;
    t.v[i] = (unsigned short)(row * 16 + col);
  }
  return t;
}
__device__ const KTab KTAB = make_ktab();

DI void k1_kgen(char* smem, int d, const u16* __restrict__ lar_p, const u16* __restrict__ aim_p,
                const u16* __restrict__ cr_p, const u16* __restrict__ ci_p, const u16* __restrict__ ldt_p,
                char* __restrict__ ws) {
  const int tid = threadIdx.x, lane = tid & 63;
  const int wv = __builtin_amdgcn_readfirstlane(tid >> 6), kq = wv & 3, role = wv >> 2;
  STAMP_BASE(ws, 0); STAMP(0);
  const int idx = d * N_ + (tid & 255);
  const float lar = bf2f(lar_p[idx]), aim = bf2f(aim_p[idx]), cr = bf2f(cr_p[idx]), ci = bf2f(ci_p[idx]);
  const float ldt = bf2f(ldt_p[d]);
  const u32x2 tab = *(const u32x2*)(&KTAB.v[tid * 4]);
  const float dt = rbf(expf(ldt));
  const float are = -rbf(expf(lar));
  const float ar = are * dt, ai = aim * dt;
  const float Cr = cr * dt, Ci = ci * dt;
  float* red = (float*)(smem + K1_LDS - 64);
  const float sw = wave_sum(2.f * (fabsf(Cr) + fabsf(Ci)));
  float wscale, winv;
  pow2_scale(sw, wscale, winv);
  if (tid < 256 && lane == 0) red[kq] = sw;
  STAMP(1);
  const float ar2 = ar * 1.4426950408889634f, air = ai * 0.15915494309189535f;
  char* wb = smem + kq * KG_WAVE + lane * 4;
  char* hb = wb + 16 * KG_RS;
  f32x2 R[8];
  R[1] = cexp_p(ar2, air, 128.f); R[2] = cexp_p(ar2, air, 256.f); R[4] = cexp_p(ar2, air, 512.f);
  const f32x2 g0 = {2.f * wscale * Cr, 2.f * wscale * Ci};
  const f32x2 g0s = cswz(g0), R2s = cswz(R[2]), R4s = cswz(R[4]);
#define CM_A(t, a, b) const f32x2 t = (f32x2){(a).x, (a).x} * (b)
#define CM_B(t, a, bs) __builtin_elementwise_fma((f32x2){(a).y, (a).y}, bs, t)
  if (role == 0) {
    f32x2 Lo[16], z[9];
    Lo[1] = cexp_p(ar2, -air, 8.f); Lo[2] = cexp_p(ar2, -air, 16.f);
    Lo[4] = cexp_p(ar2, -air, 32.f); Lo[8] = cexp_p(ar2, -air, 64.f);
    const f32x2 R8 = cexp_p(ar2, air, 1024.f), R16 = cexp_p(ar2, air, 2048.f);
    const f32x2 L2s = cswz(Lo[2]), L4s = cswz(Lo[4]), L8s = cswz(Lo[8]);
    {
      CM_A(t0, Lo[1], Lo[2]); CM_A(t4, R[1], R[2]); CM_A(t5, R8, g0); CM_A(t6, R16, g0);
      SCHED_FENCE();
      Lo[3] = CM_B(t0, Lo[1], L2s); R[3] = CM_B(t4, R[1], R2s); z[0] = CM_B(t5, R8, g0s); z[8] = CM_B(t6, R16, g0s);
      SCHED_FENCE();
    }
    {
      CM_A(t0, Lo[1], Lo[4]); CM_A(t1, Lo[2], Lo[4]); CM_A(t2, Lo[3], Lo[4]);
      CM_A(t6, R[1], R[4]); CM_A(t7, R[2], R[4]); CM_A(t8, R[3], R[4]);
      SCHED_FENCE();
      Lo[5] = CM_B(t0, Lo[1], L4s); Lo[6] = CM_B(t1, Lo[2], L4s); Lo[7] = CM_B(t2, Lo[3], L4s);
      R[5] = CM_B(t6, R[1], R4s); R[6] = CM_B(t7, R[2], R4s); R[7] = CM_B(t8, R[3], R4s);
      SCHED_FENCE();
    }
    f32x2 t[15];
    const f32x2 z0s = cswz(z[0]);
#pragma unroll
    for (int k = 1; k < 8; ++k) { t[k] = (f32x2){Lo[k].x, Lo[k].x} * Lo[8]; t[7 + k] = (f32x2){R[k].x, R[k].x} * z[0]; }
    SCHED_FENCE();
#pragma unroll
    for (int k = 1; k < 8; ++k) { Lo[8 + k] = CM_B(t[k], Lo[k], L8s); z[k] = CM_B(t[7 + k], R[k], z0s); }
    SCHED_FENCE();
    *(unsigned*)(wb) = 0x00003c00u;
#pragma unroll
    for (int k = 1; k < 16; ++k) *(unsigned*)(wb + k * KG_RS) = pk16(Lo[k]);
#pragma unroll
    for (int h = 0; h < 9; ++h) *(unsigned*)(hb + (32 + h) * KG_RS) = pk16(z[h]);
  } else {
    f32x2 g[8];
    g[0] = g0;
    const f32x2 E1 = cexp_p(ar2, air, 1.f), E2 = cexp_p(ar2, air, 2.f), E4 = cexp_p(ar2, air, 4.f);
    const f32x2 E2s = cswz(E2), E4s = cswz(E4), R1s = cswz(R[1]);
    {
      CM_A(t1, E1, g0); CM_A(t2, E2, g0); CM_A(t3, E4, g0); CM_A(t4, R[1], R[2]);
      SCHED_FENCE();
      g[1] = CM_B(t1, E1, g0s); g[2] = CM_B(t2, E2, g0s); g[4] = CM_B(t3, E4, g0s); R[3] = CM_B(t4, R[1], R2s);
      SCHED_FENCE();
    }
    {
      CM_A(t3, g[1], E2); CM_A(t4, g[1], E4); CM_A(t5, g[2], E4);
      CM_A(t6, R[1], R[4]); CM_A(t7, R[2], R[4]); CM_A(t8, R[3], R[4]);
      SCHED_FENCE();
      g[3] = CM_B(t3, g[1], E2s); g[5] = CM_B(t4, g[1], E4s); g[6] = CM_B(t5, g[2], E4s);
      R[5] = CM_B(t6, R[1], R4s); R[6] = CM_B(t7, R[2], R4s); R[7] = CM_B(t8, R[3], R4s);
      SCHED_FENCE();
    }
    g[7] = cmul(g[3], E4, E4s);
    f32x2 t[24];
    const f32x2 R3s = cswz(R[3]), g4s = cswz(g[4]);
#pragma unroll
    for (int c = 0; c < 8; ++c) t[c] = (f32x2){g[c].x, g[c].x} * R[1];
#pragma unroll
    for (int c = 0; c < 4; ++c) { t[8 + c] = (f32x2){g[2 * c].x, g[2 * c].x} * R[2]; t[12 + c] = (f32x2){g[2 * c].x, g[2 * c].x} * R[3]; }
#pragma unroll
    for (int h = 0; h < 4; ++h) { t[16 + 2 * h] = (f32x2){R[4 + h].x, R[4 + h].x} * g[0]; t[17 + 2 * h] = (f32x2){R[4 + h].x, R[4 + h].x} * g[4]; }
    SCHED_FENCE();
#pragma unroll
    for (int c = 0; c < 8; ++c) t[c] = CM_B(t[c], g[c], R1s);
#pragma unroll
    for (int c = 0; c < 4; ++c) { t[8 + c] = CM_B(t[8 + c], g[2 * c], R2s); t[12 + c] = CM_B(t[12 + c], g[2 * c], R3s); }
#pragma unroll
    for (int h = 0; h < 4; ++h) { t[16 + 2 * h] = CM_B(t[16 + 2 * h], R[4 + h], g0s); t[17 + 2 * h] = CM_B(t[17 + 2 * h], R[4 + h], g4s); }
    SCHED_FENCE();
#pragma unroll
    for (int c = 0; c < 8; ++c) *(unsigned*)(hb + c * KG_RS) = pk16(g[c]);
#pragma unroll
    for (int i = 0; i < 24; ++i) *(unsigned*)(hb + (8 + i) * KG_RS) = pk16(t[i]);
  }
  STAMP(2);
  __syncthreads();
  {
    const int r = lane & 15, gq = lane >> 4;
    const char* base = smem + kq * KG_WAVE + r * KG_RS + gq * 16;
    float* part = (float*)(smem + KG_PART) + kq;
    if (role == 0) {
      f32x4 acc[2] = {};
#pragma unroll
      for (int ks = 0; ks < 4; ++ks) {
        const f16x8 b = *(const f16x8*)(base + ks * 64);
#pragma unroll
        for (int m = 0; m < 2; ++m) {
          const f16x8 a = *(const f16x8*)(base + (16 + 16 * m) * KG_RS + ks * 64);
          acc[m] = __builtin_amdgcn_mfma_f32_16x16x32_f16(a, b, acc[m], 0, 0, 0);
        }
      }
#pragma unroll
      for (int m = 0; m < 2; ++m)
#pragma unroll
        for (int j = 0; j < 4; ++j) part[((16 * m + 4 * gq + j) * 16 + r) * 4] = acc[m][j] * winv;
    } else {
      f32x4 acc = {};
#pragma unroll
      for (int ks = 0; ks < 4; ++ks) {
        const f16x8 b = *(const f16x8*)(base + ks * 64);
        const f16x8 a = *(const f16x8*)(base + 48 * KG_RS + ks * 64);
        acc = __builtin_amdgcn_mfma_f32_16x16x32_f16(a, b, acc, 0, 0, 0);
      }
#pragma unroll
      for (int j = 0; j < 4; ++j) part[((32 + 4 * gq + j) * 16 + r) * 4] = acc[j] * winv;
    }
  }
  STAMP(3);
  __syncthreads();
  STAMP(4);
  {
    float scale, inv_scale;
    pow2_scale((red[0] + red[1]) + (red[2] + red[3]), scale, inv_scale);
    const f32x4* part = (const f32x4*)(smem + KG_PART);
    unsigned pk[2];
#pragma unroll
    for (int j = 0; j < 2; ++j) {
      const f32x4 p0 = part[tab[j] & 0xffffu], p1 = part[tab[j] >> 16];
      pk[j] = pack_f16(((p0[0] + p0[1]) + (p0[2] + p0[3])) * scale, ((p1[0] + p1[1]) + (p1[2] + p1[3])) * scale);
    }
    const __amdgpu_buffer_rsrc_t rs = ws_rsrc(ws);
    const size_t kr = OFF_KR + (size_t)d * KR_LEN * 2;
    wst64(rs, kr + tid * 8, (u32x2){pk[0], pk[1]});
    if (tid < 32) wst128(rs, kr + 4096 + tid * 16, (u32x4){0u, 0u, 0u, 0u});
    if (tid == 0) ((float*)(ws + OFF_KS))[d] = inv_scale;
  }
  STAMP(5); STAMPW(14);
}

DI float reduce4(const float (&a)[4]) {
  float v[2];
#pragma unroll
  for (int i = 0; i < 2; ++i) {
    auto r = __builtin_amdgcn_permlane32_swap(__float_as_uint(a[i]), __float_as_uint(a[i + 2]), false, false);
    v[i] = __uint_as_float(r[0]) + __uint_as_float(r[1]);
  }
  auto r = __builtin_amdgcn_permlane16_swap(__float_as_uint(v[0]), __float_as_uint(v[1]), false, false);
  float x = __uint_as_float(r[0]) + __uint_as_float(r[1]);
  x = dpp_add<0xB1>(x);
  x = dpp_add<0x4E>(x);
  x = dpp_add<0x141>(x);
  x = dpp_add<0x140>(x);
  return x;
}
DI float bcast_row(float w, int t) { return __uint_as_float(__builtin_amdgcn_readlane(__float_as_uint(w), 16 * t)); }

constexpr int LN_PL = 448 * 64 + 56 * 16;
DI void k1_ln(char* smem, int blk, const u16* __restrict__ x, char* __restrict__ ws) {
  const int tid = threadIdx.x, lane = tid & 63, wv = tid >> 6;
  STAMP_BASE(ws, 0); STAMP(0);
  const int tok0 = blk * 32 + wv * 4;
  const bool act = lane < 56;
  u32x4 xr[4];
#pragma unroll
  for (int t = 0; t < 4; ++t) {
    xr[t] = (u32x4){0u, 0u, 0u, 0u};
    if (act) xr[t] = *(const u32x4*)(x + (size_t)(tok0 + t) * D_ + lane * 8);
  }
  f32x2 xf[4][4];
  float part[4];
#pragma unroll
  for (int t = 0; t < 4; ++t) {
#pragma unroll
    for (int j = 0; j < 4; ++j) xf[t][j] = (f32x2){bflo(xr[t][j]), bfhi(xr[t][j])};
    const f32x2 s2 = (xf[t][0] + xf[t][1]) + (xf[t][2] + xf[t][3]);
    part[t] = s2.x + s2.y;
  }
  const float wm = reduce4(part) * (1.f / D_);
#pragma unroll
  for (int t = 0; t < 4; ++t) {
    const float m = bcast_row(wm, t);
    f32x2 q2 = {0.f, 0.f};
#pragma unroll
    for (int j = 0; j < 4; ++j) { const f32x2 dd = xf[t][j] - (f32x2){m, m}; q2 = __builtin_elementwise_fma(dd, dd, q2); }
    part[t] = act ? q2.x + q2.y : 0.f;
  }
  const float wq = reduce4(part);
  const float sd = rbf(sqrtf(rbf(rbf(wq * (1.f / D_)) + 1e-5f)));
  const float wr = 1.f / sd;
  const float wb = rbf(wm);
  STAMP(1);
  f32x2 un[4][4];
#pragma unroll
  for (int t = 0; t < 4; ++t) {
    const float mb = bcast_row(wb, t), ri = bcast_row(wr, t);
#pragma unroll
    for (int j = 0; j < 4; ++j) {
      const f32x2 dd = xf[t][j] - (f32x2){mb, mb};
      const unsigned pb = pack_bf16(dd.x, dd.y);
      un[t][j] = (f32x2){bflo(pb), bfhi(pb)} * (f32x2){ri, ri};
    }
  }
  if (act) {
    char* ub = smem + lane * (8 * 64 + 16) + wv * 8;
#pragma unroll
    for (int j = 0; j < 8; ++j) {
      u32x2 pu, px;
#pragma unroll
      for (int t = 0; t < 2; ++t) {
        pu[t] = pack_bf16(un[2 * t][j >> 1][j & 1], un[2 * t + 1][j >> 1][j & 1]);
        const unsigned a = xr[2 * t][j >> 1], b = xr[2 * t + 1][j >> 1];
        px[t] = (j & 1) ? __builtin_amdgcn_perm(b, a, 0x07060302u) : __builtin_amdgcn_perm(b, a, 0x05040100u);
      }
      *(u32x2*)(ub + j * 64) = pu;
      *(u32x2*)(ub + LN_PL + j * 64) = px;
    }
  }
  STAMP(2);
  __syncthreads();
  STAMP(3);
  const __amdgpu_buffer_rsrc_t wrs = ws_rsrc(ws);
#pragma unroll
  for (int i = 0; i < 4; ++i) {
    const int ch = i * 128 + (tid >> 2), v = tid & 3;
    if (ch < D_) {
      const int o = ch * 64 + (ch >> 3) * 16 + v * 16;
      const u32x4 a = *(const u32x4*)(smem + o);
      const u32x4 b = *(const u32x4*)(smem + LN_PL + o);
      wst128(wrs, OFF_UT + ((size_t)ch * L_ + blk * 32 + v * 8) * 2, a);
      wst128(wrs, OFF_XT + ((size_t)ch * L_ + blk * 32 + v * 8) * 2, b);
    }
  }
  STAMP(4); STAMPW(14);
}

__global__ __launch_bounds__(512) void k1_kernel(const u16* __restrict__ x, const u16* __restrict__ lar,
                                                 const u16* __restrict__ aim, const u16* __restrict__ cr,
                                                 const u16* __restrict__ ci, const u16* __restrict__ ldt,
                                                 char* __restrict__ ws) {
  __shared__ __attribute__((aligned(16))) char smem[K1_LDS];
  int b = blockIdx.x;
  if (b < K1_LN) {
    if (b == 0 && threadIdx.x < 32) ((unsigned*)(ws + OFF_CNT))[threadIdx.x * 64] = 0u;
    k1_ln(smem, b, x, ws);
    return;
  }
  k1_kgen(smem, b - K1_LN, lar, aim, cr, ci, ldt, ws);
}

DI void k2_wtile(char* smem, const u16* __restrict__ src, int ldn, size_t dsto, const u16* __restrict__ b1s, char* ws) {
  const int tid = threadIdx.x;
  const __amdgpu_buffer_rsrc_t rs = ws_rsrc(ws);
  STAMP_BASE(ws, 1); STAMP(0);
  u16* t = (u16*)smem;
  u32x4 v[4];
#pragma unroll
  for (int i = 0; i < 4; ++i) {
    const int c = i * 512 + tid, k = c >> 2, q = c & 3;
    if (c < 1792) v[i] = *(const u32x4*)(src + (size_t)k * ldn + q * 8);
  }
#pragma unroll
  for (int i = 0; i < 4; ++i) {
    const int c = i * 512 + tid, k = c >> 2, q = c & 3;
    if (c < 1792) {
      *(u32x2*)(t + k * 36 + q * 8) = (u32x2){v[i][0], v[i][1]};
      *(u32x2*)(t + k * 36 + q * 8 + 4) = (u32x2){v[i][2], v[i][3]};
    }
  }
  __syncthreads();
  STAMP(1);
#pragma unroll
  for (int i = 0; i < 4; ++i) {
    const int c = i * 512 + tid, ks = c >> 6, l = c & 63;
    if (c < 1792) {
      const int col = perm23(l & 31), k0 = ks * 16 + (l >> 5) * 8;
      unsigned w[4];
#pragma unroll
      for (int j = 0; j < 4; ++j)
        w[j] = (unsigned)t[(k0 + 2 * j) * 36 + col] | ((unsigned)t[(k0 + 2 * j + 1) * 36 + col] << 16);
      wst128(rs, dsto + (size_t)c * 16, (u32x4){w[0], w[1], w[2], w[3]});
    }
  }
  STAMP(2);
  if (b1s) {
    const int col = tid & 31, kg = tid >> 5;
    float s = 0.f;
#pragma unroll
    for (int k = 0; k < 28; ++k) s += bf2f(t[(kg * 28 + k) * 36 + col]);
    float* red = (float*)(smem + 448 * 72);
    red[tid] = s;
    __syncthreads();
    if (tid < 64) {
      const int c = perm23(tid & 31);
      float a = 0.f;
#pragma unroll
      for (int g = 0; g < 16; ++g) a += red[g * 32 + c];
      const float hi = rbf(a);
      const unsigned w0 = pack_bf16(hi, a - hi), w1 = (unsigned)b1s[c];
      wst128(rs, dsto + (size_t)(28 * 64 + tid) * 16, (u32x4){tid < 32 ? w0 : 0u, tid < 32 ? w1 : 0u, 0u, 0u});
    }
  }
  STAMP(3); STAMPW(14);
}

constexpr int CV_CS = 4160;
constexpr int CV_KOFF = 8 * CV_CS;
constexpr int CV_LDS = 8 * 7 * 1024;
static_assert(CV_LDS >= CV_KOFF + KR_LEN * 2, "lds");
constexpr int K2_GRID = D_ + 112;

__global__ __launch_bounds__(512) void k2_kernel(const u16* __restrict__ dskip, const u16* __restrict__ w1,
                                                 const u16* __restrict__ b1, const u16* __restrict__ w2,
                                                 char* __restrict__ ws) {
  __shared__ __attribute__((aligned(16))) char smem[CV_LDS];
  if (blockIdx.x >= D_) {
    const int b = blockIdx.x - D_;
    if (b < 56) {
      k2_wtile(smem, w1 + b * 32, F_, OFF_W1 + (size_t)b * W1_FRAGS * 1024, b1 + b * 32, ws);
    } else {
      const int dt = (b - 56) >> 2, kc = (b - 56) & 3;
      k2_wtile(smem, w2 + (size_t)kc * 448 * D_ + dt * 32, D_, OFF_W2 + ((size_t)dt * 112 + kc * 28) * 1024, nullptr, ws);
    }
    return;
  }
  const int d = blockIdx.x, tid = threadIdx.x, lane = tid & 63;
  const int wv = __builtin_amdgcn_readfirstlane(tid >> 6);
  STAMP_BASE(ws, 1); STAMP(0);
  const u16* uT = (const u16*)(ws + OFF_UT) + (size_t)d * L_;
  const u16* xT = (const u16*)(ws + OFF_XT) + (size_t)d * L_;
  const char* kr = ws + OFF_KR + (size_t)d * KR_LEN * 2;
  const int m0 = tid & 255, half = wv >> 2;
  u32x4 c0 = {0u, 0u, 0u, 0u};
  if (m0 > 0) c0 = *(const u32x4*)(uT + (m0 - 1) * 8);
  const u32x4 c1 = *(const u32x4*)(uT + m0 * 8);
  u32x4 k0 = {0u, 0u, 0u, 0u};
  if (tid < 288) k0 = *(const u32x4*)(kr + tid * 16);
  const int c16 = lane & 15, g4 = lane >> 4;
  const int pe = 256 * wv + 16 * c16 + 4 * g4;
  const u32x2 ue = *(const u32x2*)(uT + pe), xe = *(const u32x2*)(xT + pe);
  const float inv_scale = ((const float*)(ws + OFF_KS))[d];
  const float dsk = bf2f(dskip[d]);
  SCHED_FENCE();
  STAMP(1);
  if (tid < 288) *(u32x4*)(smem + CV_KOFF + tid * 16) = k0;
  if (tid < 8) *(u32x4*)(smem + tid * CV_CS) = (u32x4){0u, 0u, 0u, 0u};
  {
    unsigned p[8];
#pragma unroll
    for (int j = 0; j < 4; ++j) {
      p[j] = pack_f16(bflo(c0[j]), bfhi(c0[j]));
      p[4 + j] = pack_f16(bflo(c1[j]), bfhi(c1[j]));
    }
    char* dst = smem + (m0 + 1) * 16 + half * 4 * CV_CS;
    if (half == 0) {
      unsigned q[5];
#pragma unroll
      for (int i = 0; i < 5; ++i) q[i] = __builtin_amdgcn_alignbit(p[i + 1], p[i], 16);
      *(u32x4*)(dst) = (u32x4){q[0], q[1], q[2], q[3]};
      *(u32x4*)(dst + CV_CS) = (u32x4){p[1], p[2], p[3], p[4]};
      *(u32x4*)(dst + 2 * CV_CS) = (u32x4){q[1], q[2], q[3], q[4]};
      *(u32x4*)(dst + 3 * CV_CS) = (u32x4){p[2], p[3], p[4], p[5]};
    } else {
      unsigned q[5];
#pragma unroll
      for (int i = 0; i < 5; ++i) q[i] = __builtin_amdgcn_alignbit(p[i + 3], p[i + 2], 16);
      *(u32x4*)(dst) = (u32x4){q[0], q[1], q[2], q[3]};
      *(u32x4*)(dst + CV_CS) = (u32x4){p[3], p[4], p[5], p[6]};
      *(u32x4*)(dst + 2 * CV_CS) = (u32x4){q[1], q[2], q[3], q[4]};
      *(u32x4*)(dst + 3 * CV_CS) = (u32x4){p[4], p[5], p[6], p[7]};
    }
  }
  STAMP(2);
  __syncthreads();
  STAMP(3);
  f32x4 acc[8] = {};
  {
    const char* abase = smem + (c16 & 7) * CV_CS + ((c16 & 8) + 8 * g4) * 2 + wv * 64;
    const char* bbase = smem + CV_KOFF + (2032 - 16 * c16 + 8 * g4) * 2 + wv * 64;
    f16x8 A[8], B[8];
#pragma unroll
    for (int i = 0; i < 8; ++i) {
      A[i] = *(const f16x8*)(abase + i * 512);
      B[i] = *(const f16x8*)(bbase - i * 512);
    }
#pragma unroll
    for (int t = 0; t < 8; ++t)
#pragma unroll
      for (int ia = 0; ia <= t; ++ia)
        acc[t] = __builtin_amdgcn_mfma_f32_16x16x32_f16(A[ia], B[t - ia], acc[t], 0, 0, 0);
  }
  STAMP(4);
  __syncthreads();
  STAMP(5);
  f32x4* red = (f32x4*)smem;
  f32x4 own = acc[0];
#pragma unroll
  for (int t = 0; t < 8; ++t) {
    if (t == wv) own = acc[t];
    else red[(t * 7 + (wv > t ? wv - 1 : wv)) * 64 + lane] = acc[t];
  }
  __syncthreads();
  {
    f32x4 s[7];
#pragma unroll
    for (int i = 0; i < 7; ++i) s[i] = red[(wv * 7 + i) * 64 + lane];
    const f32x4 sum = ((own + s[0]) + (s[1] + s[2])) + ((s[3] + s[4]) + (s[5] + s[6]));
    float h[4];
#pragma unroll
    for (int j = 0; j < 4; ++j) {
      const float uu = (j & 1) ? bfhi(ue[j >> 1]) : bflo(ue[j >> 1]);
      const float xx = (j & 1) ? bfhi(xe[j >> 1]) : bflo(xe[j >> 1]);
      const float y = rbf(sum[j] * inv_scale);
      const float tt = rbf(y + rbf(uu * dsk));
      h[j] = xx + tt;
    }
    const __amdgpu_buffer_rsrc_t rs = ws_rsrc(ws);
    wst64(rs, OFF_HT + ((size_t)d * HT_LD + pe) * 2, (u32x2){pack_bf16(h[0], h[1]), pack_bf16(h[2], h[3])});
  }
  STAMP(6); STAMPW(14);
}

constexpr int K3_TILE = 448 * 128;
constexpr int K3_RED = 8 * 7 * 2048;
constexpr int K3_STAT = K3_RED;
constexpr int K3_LDS = K3_STAT + 4 * 64 * 8;
typedef __attribute__((address_space(3))) bf16x4* lds_bf16x4_p;
typedef __attribute__((address_space(1))) unsigned gu32;

DI bf16x8 k3_bfrag(const char* smem, unsigned lane_off0, unsigned lane_off1, int off) {
  const bf16x4 lo = __builtin_amdgcn_ds_read_tr16_b64_v4bf16((lds_bf16x4_p)(smem + lane_off0 + off));
  const bf16x4 hi = __builtin_amdgcn_ds_read_tr16_b64_v4bf16((lds_bf16x4_p)(smem + lane_off1 + off));
  return __builtin_shufflevector(lo, hi, 0, 1, 2, 3, 4, 5, 6, 7);
}
#define K34_WIN 3
#define W2_A 4
#define H1_WIN 6
template <int N> DI void wait_vm() { asm volatile("s_waitcnt vmcnt(%0)" ::"i"(N) : "memory"); }

__global__ __launch_bounds__(512, 2) void k34_kernel(const u16* __restrict__ b2, char* __restrict__ ws,
                                                     u16* __restrict__ out) {
  __shared__ __attribute__((aligned(16))) char smem[K3_LDS];
  const int tid = threadIdx.x, lane = tid & 63;
  const int wv = __builtin_amdgcn_readfirstlane(tid >> 6);
  const int xcd = blockIdx.x & 7, idx = blockIdx.x >> 3;
  const int grp = 4 * xcd + idx / 7, j = idx % 7;
  const int tok0 = grp * 64, ft = j * 8 + wv;
  const int sh = (wv >> 1) & 1;
  const int s2 = wv >> 2;
  STAMP_BASE(ws, 2); STAMP(0);
  const u16* hsrc = (const u16*)(ws + OFF_HT) + (size_t)(tid >> 3) * HT_LD + tok0 + (tid & 7) * 8;
  const bf16x8* w1f = (const bf16x8*)(ws + OFF_W1) + (size_t)ft * W1_FRAGS * 64 + lane;
  const bf16x8* w1a = w1f + sh * 128;
  const bf16x8* w1b = w1f + (1 - sh) * 128;
  const bf16x8* wa = (const bf16x8*)(ws + OFF_W2) + ((size_t)(2 * j) * 112 + wv * 14) * 64 + lane;
  u32x4 st[7];
  bf16x8 A[28];
  bf16x8 a0[14], a1[14];
#define ISSUE1(c) do { st[c] = *(const u32x4*)(hsrc + (size_t)(64 * (c)) * HT_LD); \
    A[4 * (c)] = w1a[(4 * (c)) * 64]; A[4 * (c) + 1] = w1a[(4 * (c) + 1) * 64]; \
    A[4 * (c) + 2] = w1b[(4 * (c)) * 64]; A[4 * (c) + 3] = w1b[(4 * (c) + 1) * 64]; } while (0)
#define ISSUE2(c) do { a0[2 * (c)] = wa[(2 * (c)) * 64]; a1[2 * (c)] = wa[(112 + 2 * (c)) * 64]; \
    a0[2 * (c) + 1] = wa[(2 * (c) + 1) * 64]; a1[2 * (c) + 1] = wa[(112 + 2 * (c) + 1) * 64]; } while (0)
#pragma unroll
  for (int c = 0; c < K34_WIN; ++c) { ISSUE1(c); SCHED_FENCE(); }
  const bf16x8 Aext = w1f[28 * 64];
  SCHED_FENCE();
  const int hh = lane >> 5, col = lane & 31;
  const unsigned q = (lane & 15) >> 2, p = lane & 3, blk = (lane >> 4) & 1;
  char* lw = smem + 128 * (tid >> 3) + 16 * ((tid & 7) ^ (((tid >> 4) & 1) << 2));
  unsigned oa[2][2], ob[2][2];
  {
    const unsigned r0 = 8 * hh + q, r1 = 8 * hh + 4 + q, sw = ((q >> 1) & 1) << 2;
#pragma unroll
    for (int it = 0; it < 2; ++it) {
      const unsigned c = (wv & 1) ^ it;
      const unsigned chk = (4 * c + 2 * blk + (p >> 1)) ^ sw;
      const unsigned x0 = 128 * r0 + 16 * chk + 8 * (p & 1), x1 = 128 * r1 + 16 * chk + 8 * (p & 1);
      oa[it][0] = x0 + sh * 4096; oa[it][1] = x1 + sh * 4096;
      ob[it][0] = x0 + (1 - sh) * 4096; ob[it][1] = x1 + (1 - sh) * 4096;
    }
  }
  f32x16 acc[2] = {};
  float ssum = 0.f, ssq = 0.f;
  const bf16x2 ones2 = {(__bf16)1.0f, (__bf16)1.0f};
#define WRITE1(c) do { *(u32x4*)(lw + (c) * 8192) = st[c]; } while (0)
#define RDB(n, it) ((((n) & 2) == 0) ? k3_bfrag(smem, oa[it][0], oa[it][1], ((n) >> 2) * 8192 + ((n) & 1) * 2048) \
                                      : k3_bfrag(smem, ob[it][0], ob[it][1], ((n) >> 2) * 8192 + ((n) & 1) * 2048))
  bf16x8 Bf[3][2];
  WRITE1(0);
  WRITE1(1);
  __syncthreads();
  STAMP(1);
  Bf[0][0] = RDB(0, 0); Bf[0][1] = RDB(0, 1); Bf[1][0] = RDB(1, 0); Bf[1][1] = RDB(1, 1);
#pragma unroll
  for (int n = 0; n < 28; ++n) {
    const int c = n >> 2, i = n & 3;
    acc[0] = __builtin_amdgcn_mfma_f32_32x32x16_bf16(A[n], Bf[n % 3][0], acc[0], 0, 0, 0);
    if (n + 2 < 28) Bf[(n + 2) % 3][0] = RDB(n + 2, 0);
    if (i < 2) {
      const u32x4 B = __builtin_bit_cast(u32x4, Bf[n % 3][0]);
      const bf16x2 pr = __builtin_bit_cast(bf16x2, s2 ? B[2] : B[0]);
      ssum = __builtin_amdgcn_fdot2_f32_bf16(pr, ones2, ssum, false);
      ssq = __builtin_amdgcn_fdot2_f32_bf16(pr, pr, ssq, false);
    }
    SCHED_FENCE();
    acc[1] = __builtin_amdgcn_mfma_f32_32x32x16_bf16(A[n], Bf[n % 3][1], acc[1], 0, 0, 0);
    if (n + 2 < 28) Bf[(n + 2) % 3][1] = RDB(n + 2, 1);
    if (i < 2) {
      const u32x4 B = __builtin_bit_cast(u32x4, Bf[n % 3][0]);
      const bf16x2 pr = __builtin_bit_cast(bf16x2, s2 ? B[3] : B[1]);
      ssum = __builtin_amdgcn_fdot2_f32_bf16(pr, ones2, ssum, false);
      ssq = __builtin_amdgcn_fdot2_f32_bf16(pr, pr, ssq, false);
    }
    if (i == 0 && c + 2 < 7) WRITE1(c + 2);
    if (i == 2) { if (c + K34_WIN < 7) ISSUE1(c + K34_WIN); else if (c + K34_WIN - 7 < W2_A) ISSUE2(c + K34_WIN - 7); }
    if (n == 27) { _Pragma("unroll") for (int z_ = K34_WIN; z_ < W2_A; ++z_) ISSUE2(z_); }
    SCHED_FENCE();
    if (i == 3 && c + 2 < 7) __syncthreads();
  }
  STAMP(2);
  float2* stats = (float2*)(smem + K3_STAT);
  {
    auto r1 = __builtin_amdgcn_permlane32_swap(__float_as_uint(ssum), __float_as_uint(ssum), false, false);
    ssum = __uint_as_float(r1[0]) + __uint_as_float(r1[1]);
    auto r2 = __builtin_amdgcn_permlane32_swap(__float_as_uint(ssq), __float_as_uint(ssq), false, false);
    ssq = __uint_as_float(r2[0]) + __uint_as_float(r2[1]);
    if (hh == 0) stats[(wv >> 1) * 64 + (wv & 1) * 32 + col] = make_float2(ssum, ssq);
  }
  const int t2 = wv >> 1, hf2 = wv & 1;
  const int c2 = t2 & 1;
  bf16x8 res;
  {
    const unsigned r0 = 8 * hh + q, r1 = 8 * hh + 4 + q, sw = ((q >> 1) & 1) << 2;
    const unsigned chk = (4 * c2 + 2 * blk + (p >> 1)) ^ sw;
    res = k3_bfrag(smem, 128 * r0 + 16 * chk + 8 * (p & 1), 128 * r1 + 16 * chk + 8 * (p & 1), (j * 4 + (t2 >> 1) * 2 + hf2) * 2048);
  }
  __syncthreads();
  float sdv[2], c1v[2], c2v[2];
#pragma unroll
  for (int it = 0; it < 2; ++it) {
    const unsigned c = (wv & 1) ^ it;
    const float2 s0 = stats[c * 32 + col], s1 = stats[64 + c * 32 + col], s2_ = stats[128 + c * 32 + col], s3 = stats[192 + c * 32 + col];
    const float mean = ((s0.x + s1.x) + (s2_.x + s3.x)) * (1.f / D_);
    const float var = fmaxf(__builtin_fmaf(-mean, mean, ((s0.y + s1.y) + (s2_.y + s3.y)) * (1.f / D_)), 0.f);
    const float sd = rbf(sqrtf(rbf(rbf(var) + 1e-5f)));
    const float rs = __builtin_amdgcn_rcpf(sd);
    sdv[it] = sd;
    c1v[it] = rs * -2.3022082f;
    c2v[it] = rs * rs * rs * -0.10294324f;
    const unsigned w0 = pack_bf16(-mean, -mean), w1 = pack_bf16(sd, 0.f);
    const u32x4 bw = {hh ? 0u : w0, hh ? 0u : w1, 0u, 0u};
    acc[it] = __builtin_amdgcn_mfma_f32_32x32x16_bf16(Aext, __builtin_bit_cast(bf16x8, bw), acc[it], 0, 0, 0);
  }
  const __amdgpu_buffer_rsrc_t h1rs = __builtin_amdgcn_make_buffer_rsrc((void*)(ws + OFF_H1), 0, L_ * F_ * 2, 0x00020000);
#pragma unroll
  for (int it = 0; it < 2; ++it) {
    const unsigned c = (wv & 1) ^ it;
    const unsigned fb = (((grp * 2 + c) * 112 + 2 * ft) * 64 + lane) * 16;
    const f32x2 c1p = {c1v[it], c1v[it]}, c2p = {c2v[it], c2v[it]}, sdp = {sdv[it], sdv[it]};
#pragma unroll
    for (int hf = 0; hf < 2; ++hf) {
      f32x2 a[4], z[4];
#pragma unroll
      for (int e = 0; e < 4; ++e) a[e] = (f32x2){acc[it][8 * hf + 2 * e], acc[it][8 * hf + 2 * e + 1]};
#pragma unroll
      for (int e = 0; e < 4; ++e) z[e] = a[e] * a[e];
#pragma unroll
      for (int e = 0; e < 4; ++e) z[e] = __builtin_elementwise_fma(z[e], c2p, c1p);
#pragma unroll
      for (int e = 0; e < 4; ++e) z[e] = a[e] * z[e];
#pragma unroll
      for (int e = 0; e < 4; ++e) z[e] = (f32x2){__builtin_amdgcn_exp2f(z[e].x), __builtin_amdgcn_exp2f(z[e].y)};
#pragma unroll
      for (int e = 0; e < 4; ++e) z[e] = __builtin_elementwise_fma(z[e], sdp, sdp);
#pragma unroll
      for (int e = 0; e < 4; ++e) z[e] = (f32x2){__builtin_amdgcn_rcpf(z[e].x), __builtin_amdgcn_rcpf(z[e].y)};
#pragma unroll
      for (int e = 0; e < 4; ++e) z[e] = a[e] * z[e];
      const u32x4 pk = {pack_bf16(z[0].x, z[0].y), pack_bf16(z[1].x, z[1].y), pack_bf16(z[2].x, z[2].y), pack_bf16(z[3].x, z[3].y)};
      __builtin_amdgcn_raw_buffer_store_b128(pk, h1rs, fb + hf * 1024, 0, 16);
      SCHED_FENCE();
    }
  }
  STAMP(3);
  wait_vm<0>();
  __builtin_amdgcn_s_barrier();
  asm volatile("" ::: "memory");
  STAMP(4);
  gu32* cnt = (gu32*)(ws + OFF_CNT) + grp * 64;
  if (wv == 0) {
    if (lane == 0) {
      __hip_atomic_fetch_add(cnt, 1u, __ATOMIC_RELAXED, __HIP_MEMORY_SCOPE_AGENT);
      for (unsigned spins = 0; spins < (1u << 20); ++spins) {
        if (__hip_atomic_load(cnt, __ATOMIC_RELAXED, __HIP_MEMORY_SCOPE_AGENT) >= 7u) break;
        __builtin_amdgcn_s_sleep(1);
      }
    }
    __builtin_amdgcn_fence(__ATOMIC_ACQUIRE, "wavefront");
  }
  SCHED_FENCE();
#pragma unroll
  for (int z_ = W2_A; z_ < 7; ++z_) ISSUE2(z_);
  SCHED_FENCE();
  __builtin_amdgcn_s_barrier();
  __builtin_amdgcn_fence(__ATOMIC_ACQUIRE, "wavefront");
  asm volatile("" ::: "memory");
  STAMP(5);
  const unsigned hb = (((grp * 2) * 112 + wv * 14) * 64 + lane) * 16;
  u32x4 b0[H1_WIN], b1v[H1_WIN];
#pragma unroll
  for (int i = 0; i < H1_WIN; ++i) {
    b0[i] = __builtin_amdgcn_raw_buffer_load_b128(h1rs, hb + i * 1024, 0, 16);
    b1v[i] = __builtin_amdgcn_raw_buffer_load_b128(h1rs, hb + (112 + i) * 1024, 0, 16);
  }
  const int d0 = j * 64 + (t2 >> 1) * 32 + 16 * hf2 + 8 * hh;
  const u32x4 bz = *(const u32x4*)(b2 + d0);
  SCHED_FENCE();
  f32x16 acc2[4] = {};
#pragma unroll
  for (int ks = 0; ks < 14; ++ks) {
    const int sl = ks % H1_WIN;
    const bf16x8 y0 = __builtin_bit_cast(bf16x8, b0[sl]), y1 = __builtin_bit_cast(bf16x8, b1v[sl]);
    acc2[0] = __builtin_amdgcn_mfma_f32_32x32x16_bf16(a0[ks], y0, acc2[0], 0, 0, 0);
    acc2[1] = __builtin_amdgcn_mfma_f32_32x32x16_bf16(a0[ks], y1, acc2[1], 0, 0, 0);
    acc2[2] = __builtin_amdgcn_mfma_f32_32x32x16_bf16(a1[ks], y0, acc2[2], 0, 0, 0);
    acc2[3] = __builtin_amdgcn_mfma_f32_32x32x16_bf16(a1[ks], y1, acc2[3], 0, 0, 0);
    if (ks + H1_WIN < 14) {
      b0[sl] = __builtin_amdgcn_raw_buffer_load_b128(h1rs, hb + (ks + H1_WIN) * 1024, 0, 16);
      b1v[sl] = __builtin_amdgcn_raw_buffer_load_b128(h1rs, hb + (112 + ks + H1_WIN) * 1024, 0, 16);
    }
    SCHED_FENCE();
  }
  STAMP(6);
  f32x4* red = (f32x4*)smem;
  f32x4 own0 = {}, own1 = {};
#pragma unroll
  for (int u = 0; u < 8; ++u) {
    const f32x4 v0 = {acc2[u >> 1][8 * (u & 1)], acc2[u >> 1][8 * (u & 1) + 1], acc2[u >> 1][8 * (u & 1) + 2], acc2[u >> 1][8 * (u & 1) + 3]};
    const f32x4 v1 = {acc2[u >> 1][8 * (u & 1) + 4], acc2[u >> 1][8 * (u & 1) + 5], acc2[u >> 1][8 * (u & 1) + 6], acc2[u >> 1][8 * (u & 1) + 7]};
    if (u == wv) { own0 = v0; own1 = v1; }
    else {
      const int slot = u * 7 + (wv > u ? wv - 1 : wv);
      red[(slot * 2) * 64 + lane] = v0;
      red[(slot * 2 + 1) * 64 + lane] = v1;
    }
  }
  __syncthreads();
  STAMP(7);
  {
    f32x4 s0[7], s1[7];
#pragma unroll
    for (int i = 0; i < 7; ++i) { s0[i] = red[((wv * 7 + i) * 2) * 64 + lane]; s1[i] = red[((wv * 7 + i) * 2 + 1) * 64 + lane]; }
    const f32x4 o0 = ((own0 + s0[0]) + (s0[1] + s0[2])) + ((s0[3] + s0[4]) + (s0[5] + s0[6]));
    const f32x4 o1 = ((own1 + s1[0]) + (s1[1] + s1[2])) + ((s1[3] + s1[4]) + (s1[5] + s1[6]));
    const u32x4 hr = __builtin_bit_cast(u32x4, res);
    unsigned pk[4];
#pragma unroll
    for (int e = 0; e < 4; ++e) {
      const float x0 = (e < 2) ? o0[2 * e] : o1[2 * e - 4], x1 = (e < 2) ? o0[2 * e + 1] : o1[2 * e - 3];
      const unsigned ob_ = pack_bf16(x0, x1);
      const unsigned o2 = pack_bf16(bflo(ob_) + bflo(bz[e]), bfhi(ob_) + bfhi(bz[e]));
      pk[e] = pack_bf16(bflo(hr[e]) + bflo(o2), bfhi(hr[e]) + bfhi(o2));
    }
    *(u32x4*)(out + (size_t)(tok0 + c2 * 32 + col) * D_ + d0) = (u32x4){pk[0], pk[1], pk[2], pk[3]};
  }
  STAMP(8); STAMPW(14);
}

extern "C" void kernel_launch(void* const* d_in, const int* in_sizes, int n_in, void* d_out, int out_size, void* d_ws,
                              size_t ws_size, hipStream_t stream) {
  const u16* x = (const u16*)d_in[0];
  const u16* lar = (const u16*)d_in[1];
  const u16* aim = (const u16*)d_in[2];
  const u16* cr = (const u16*)d_in[3];
  const u16* ci = (const u16*)d_in[4];
  const u16* ldt = (const u16*)d_in[5];
  const u16* dsk = (const u16*)d_in[6];
  const u16* w1 = (const u16*)d_in[8];
  const u16* b1 = (const u16*)d_in[9];
  const u16* w2 = (const u16*)d_in[10];
  const u16* b2 = (const u16*)d_in[11];
  char* ws = (char*)d_ws;
  k1_kernel<<<K1_GRID, 512, 0, stream>>>(x, lar, aim, cr, ci, ldt, ws);
  k2_kernel<<<K2_GRID, 512, 0, stream>>>(dsk, w1, b1, w2, ws);
  k34_kernel<<<224, 512, 0, stream>>>(b2, ws, (u16*)d_out);
}
```

```cpp
#include <hip/hip_runtime.h>
#include <hip/hip_bf16.h>
#include <stdint.h>

typedef __attribute__((ext_vector_type(8))) __bf16 bf16x8;
typedef __attribute__((ext_vector_type(4))) __bf16 bf16x4;
typedef __attribute__((ext_vector_type(2))) __bf16 bf16x2;
typedef __attribute__((ext_vector_type(8))) _Float16 f16x8;
typedef __attribute__((ext_vector_type(2))) _Float16 f16x2;
typedef __attribute__((ext_vector_type(2))) float f32x2;
typedef __attribute__((ext_vector_type(4))) float f32x4;
typedef __attribute__((ext_vector_type(16))) float f32x16;
typedef __attribute__((ext_vector_type(4))) unsigned u32x4;
typedef __attribute__((ext_vector_type(2))) unsigned u32x2;
typedef unsigned short u16;

#define DI __device__ __forceinline__
#define SCHED_FENCE() __builtin_amdgcn_sched_barrier(0)
#define STAMP_BASE(ws, kid)
#define STAMP(i)
#define STAMPW(i)

constexpr int L_ = 2048, D_ = 448, N_ = 256, F_ = 1792;
constexpr int KR_LEN = 2304;

constexpr size_t OFF_UT = 0;
constexpr size_t OFF_XT = OFF_UT + (size_t)D_ * L_ * 2;
constexpr size_t OFF_KR = OFF_XT + (size_t)D_ * L_ * 2;
constexpr size_t OFF_KS = OFF_KR + (size_t)D_ * KR_LEN * 2;
constexpr int W1_FRAGS = 29;
constexpr size_t OFF_CNT = OFF_KS + 2048;
constexpr size_t OFF_W1 = OFF_CNT + 8192;
constexpr size_t OFF_W2 = OFF_W1 + (size_t)56 * W1_FRAGS * 1024;
constexpr int HT_LD = L_ + 128;
constexpr size_t OFF_HT = OFF_W2 + (size_t)D_ * F_ * 2;
constexpr size_t OFF_H1 = OFF_HT + (size_t)D_ * HT_LD * 2;
constexpr size_t WS_END = OFF_H1 + (size_t)L_ * F_ * 2;

DI float bf2f(u16 v) { return __uint_as_float(((unsigned)v) << 16); }
DI float bflo(unsigned p) { return __uint_as_float(p << 16); }
DI float bfhi(unsigned p) { return __uint_as_float(p & 0xffff0000u); }
DI unsigned pack_bf16(float a, float b) {
  f32x2 v = {a, b};
  return __builtin_bit_cast(unsigned, __builtin_convertvector(v, bf16x2));
}
DI unsigned pack_f16(float a, float b) {
  f32x2 v = {a, b};
  return __builtin_bit_cast(unsigned, __builtin_convertvector(v, f16x2));
}
DI float rbf(float a) { return bflo(pack_bf16(a, a)); }
template <int CTRL> DI float dpp_add(float v) {
  const int t = __builtin_amdgcn_update_dpp(0, __float_as_int(v), CTRL, 0xf, 0xf, true);
  return v + __int_as_float(t);
}
DI float wave_sum(float v) {
  v = dpp_add<0xB1>(v);
  v = dpp_add<0x4E>(v);
  v = dpp_add<0x141>(v);
  v = dpp_add<0x140>(v);
  auto r = __builtin_amdgcn_permlane16_swap(__float_as_uint(v), __float_as_uint(v), false, false);
  v = __uint_as_float(r[0]) + __uint_as_float(r[1]);
  auto r2 = __builtin_amdgcn_permlane32_swap(__float_as_uint(v), __float_as_uint(v), false, false);
  return __uint_as_float(r2[0]) + __uint_as_float(r2[1]);
}
#define LN_AUX 0
#define WT_AUX 16
DI __amdgpu_buffer_rsrc_t ws_rsrc(char* ws) { return __builtin_amdgcn_make_buffer_rsrc((void*)ws, 0, (int)WS_END, 0x00020000); }
DI void wst128(__amdgpu_buffer_rsrc_t rs, size_t off, u32x4 v) { __builtin_amdgcn_raw_buffer_store_b128(v, rs, (unsigned)off, 0, WT_AUX); }
DI void wst64(__amdgpu_buffer_rsrc_t rs, size_t off, u32x2 v) { __builtin_amdgcn_raw_buffer_store_b64(v, rs, (unsigned)off, 0, WT_AUX); }
DI f32x2 cswz(f32x2 b) { return (f32x2){-b.y, b.x}; }
DI f32x2 cmul(f32x2 a, f32x2 b, f32x2 bs) {
  const f32x2 t = (f32x2){a.x, a.x} * b;
  return __builtin_elementwise_fma((f32x2){a.y, a.y}, bs, t);
}
DI f32x2 csq(f32x2 a) { return cmul(a, a, cswz(a)); }
DI f32x2 cexp_p(float ar2, float air, float p) {
  const float mag = __builtin_amdgcn_exp2f(ar2 * p);
  const float ph = __builtin_amdgcn_fractf(air * p);
  return (f32x2){mag * __builtin_amdgcn_cosf(ph), mag * __builtin_amdgcn_sinf(ph)};
}
DI unsigned pk16(f32x2 v) { return __builtin_bit_cast(unsigned, __builtin_convertvector(v, f16x2)); }
DI void pow2_scale(float s, float& scale, float& inv) {
  int ex = (s > 0.f) ? __builtin_amdgcn_frexp_expf(s) : 14;
  ex = min(max(ex, -100), 120);
  scale = ldexpf(1.f, 14 - ex);
  inv = ldexpf(1.f, ex - 14);
}
DI int perm23(int r) { return (r & ~12) | ((r & 4) << 1) | ((r & 8) >> 1); }

constexpr int K1_KGEN = 448, K1_LN = 64;
constexpr int K1_GRID = K1_KGEN + K1_LN;
constexpr int KG_RS = 272;
constexpr int KG_ROWS = 16 + 48;
constexpr int KG_WAVE = (16 + 41) * KG_RS;
constexpr int KG_PART = 4 * KG_WAVE + 7 * KG_RS + 64;
constexpr int K1_LDS = KG_PART + 4 * 48 * 16 * 4 + 64;

struct KTab { unsigned short v[2048]; };
constexpr KTab make_ktab() {
  KTab t{};
  for (int i = 0; i < 2048; ++i) {
    const int l = 2047 - i;
    int e = 0;
    while ((l >> (8 + e)) != 0) ++e;
    int p = l;
    if (e > 0) {
      const int q = l >> e, rem = l & ((1 << e) - 1), half = 1 << (e - 1);
      p = (q + ((rem > half || (rem == half && (q & 1))) ? 1 : 0)) << e;
    }
    int e2 = 0;
    while ((p >> (8 + e2)) != 0) ++e2;
    if (e2 > 3) e2 = 3;
    const int row = 8 * e2 + ((p >> 7) << (3 - e2)) + ((p & 7) >> e2);
    const int col = (p & 127) >> 3;
    t.v[i] = (unsigned short)(row * 16 + col);
  }
  return t;
}
__device__ const KTab KTAB = make_ktab();

DI void k1_kgen(char* smem, int d, const u16* __restrict__ lar_p, const u16* __restrict__ aim_p,
                const u16* __restrict__ cr_p, const u16* __restrict__ ci_p, const u16* __restrict__ ldt_p,
                const u16* __restrict__ w1, const u16* __restrict__ w2, char* __restrict__ ws) {
  const int tid = threadIdx.x, lane = tid & 63;
  const int kq = __builtin_amdgcn_readfirstlane(tid >> 6);
  STAMP_BASE(ws, 0); STAMP(0);
  const int idx = d * N_ + tid;
  const float lar = bf2f(lar_p[idx]), aim = bf2f(aim_p[idx]), cr = bf2f(cr_p[idx]), ci = bf2f(ci_p[idx]);
  const float ldt = bf2f(ldt_p[d]);
  const u32x4 tab = *(const u32x4*)(&KTAB.v[tid * 8]);
  unsigned pf = 0;
  if (tid < 56) pf = *(const unsigned*)(w1 + ((size_t)d * 56 + tid) * 32) ^ *(const unsigned*)(w2 + ((size_t)d * 56 + tid) * 32);
  const float LOG2E = 1.4426950408889634f;
  const float dt = rbf(__builtin_amdgcn_exp2f(ldt * LOG2E));
  const float are = -rbf(__builtin_amdgcn_exp2f(lar * LOG2E));
  const float ar = are * dt, ai = aim * dt;
  const float Cr = cr * dt, Ci = ci * dt;
  float* red = (float*)(smem + K1_LDS - 64);
  const float sw = wave_sum(2.f * (fabsf(Cr) + fabsf(Ci)));
  float wscale, winv;
  pow2_scale(sw, wscale, winv);
  if (lane == 0) red[kq] = sw;
  STAMP(1);
  const float ar2 = ar * LOG2E, air = ai * 0.15915494309189535f;
  char* wb = smem + kq * KG_WAVE + lane * 4;
  char* hb = wb + 16 * KG_RS;
#define CM_A(t, a, b) const f32x2 t = (f32x2){(a).x, (a).x} * (b)
#define CM_B(t, a, bs) __builtin_elementwise_fma((f32x2){(a).y, (a).y}, bs, t)
  f32x2 Lo[16], g[8], R[8], z[9];
  const f32x2 E1 = cexp_p(ar2, air, 1.f);
  Lo[1] = cexp_p(ar2, -air, 8.f);
  R[1] = cexp_p(ar2, air, 128.f);
  g[0] = (f32x2){2.f * wscale * Cr, 2.f * wscale * Ci};
  const f32x2 g0s = cswz(g[0]), R1s = cswz(R[1]);
  f32x2 E2, E4, R8, R16;
  {
    const f32x2 E1s = cswz(E1), L1s = cswz(Lo[1]);
    CM_A(t0, E1, E1); CM_A(t1, Lo[1], Lo[1]); CM_A(t2, R[1], R[1]); CM_A(t3, E1, g[0]);
    SCHED_FENCE();
    E2 = CM_B(t0, E1, E1s); Lo[2] = CM_B(t1, Lo[1], L1s); R[2] = CM_B(t2, R[1], R1s); g[1] = CM_B(t3, E1, g0s);
    SCHED_FENCE();
  }
  const f32x2 E2s = cswz(E2), L2s = cswz(Lo[2]), R2s = cswz(R[2]);
  {
    CM_A(t0, E2, E2); CM_A(t1, Lo[2], Lo[2]); CM_A(t2, R[2], R[2]); CM_A(t3, g[0], E2); CM_A(t4, g[1], E2);
    CM_A(t5, Lo[1], Lo[2]); CM_A(t6, R[1], R[2]);
    SCHED_FENCE();
    E4 = CM_B(t0, E2, E2s); Lo[4] = CM_B(t1, Lo[2], L2s); R[4] = CM_B(t2, R[2], R2s); g[2] = CM_B(t3, g[0], E2s);
    g[3] = CM_B(t4, g[1], E2s); Lo[3] = CM_B(t5, Lo[1], L2s); R[3] = CM_B(t6, R[1], R2s);
    SCHED_FENCE();
  }
  const f32x2 E4s = cswz(E4), L4s = cswz(Lo[4]), R4s = cswz(R[4]);
  {
    f32x2 t[12];
#pragma unroll
    for (int k = 0; k < 4; ++k) {
      t[k] = (f32x2){g[k].x, g[k].x} * E4;
      t[4 + k] = (f32x2){Lo[k + 1].x, Lo[k + 1].x} * Lo[4];
      t[8 + k] = (f32x2){R[k + 1].x, R[k + 1].x} * R[4];
    }
    SCHED_FENCE();
#pragma unroll
    for (int k = 0; k < 4; ++k) {
      g[4 + k] = CM_B(t[k], g[k], E4s);
      Lo[5 + k] = CM_B(t[4 + k], Lo[k + 1], L4s);
      if (k < 3) R[5 + k] = CM_B(t[8 + k], R[k + 1], R4s); else R8 = CM_B(t[8 + k], R[4], R4s);
    }
    SCHED_FENCE();
  }
  {
    const f32x2 L8s = cswz(Lo[8]), R8s = cswz(R8), R3s = cswz(R[3]), g4s = cswz(g[4]);
    f32x2 t[33];
#pragma unroll
    for (int k = 1; k < 8; ++k) t[24 + k] = (f32x2){Lo[k].x, Lo[k].x} * Lo[8];
    t[24] = (f32x2){g[0].x, g[0].x} * R8;
    t[32] = (f32x2){R8.x, R8.x} * R8;
#pragma unroll
    for (int c = 0; c < 8; ++c) t[c] = (f32x2){g[c].x, g[c].x} * R[1];
#pragma unroll
    for (int c = 0; c < 4; ++c) { t[8 + c] = (f32x2){g[2 * c].x, g[2 * c].x} * R[2]; t[12 + c] = (f32x2){g[2 * c].x, g[2 * c].x} * R[3]; }
#pragma unroll
    for (int h = 0; h < 4; ++h) { t[16 + 2 * h] = (f32x2){R[4 + h].x, R[4 + h].x} * g[0]; t[17 + 2 * h] = (f32x2){R[4 + h].x, R[4 + h].x} * g[4]; }
    SCHED_FENCE();
    z[0] = CM_B(t[24], g[0], R8s);
    R16 = CM_B(t[32], R8, R8s);
#pragma unroll
    for (int k = 1; k < 8; ++k) Lo[8 + k] = CM_B(t[24 + k], Lo[k], L8s);
#pragma unroll
    for (int c = 0; c < 8; ++c) t[c] = CM_B(t[c], g[c], R1s);
#pragma unroll
    for (int c = 0; c < 4; ++c) { t[8 + c] = CM_B(t[8 + c], g[2 * c], R2s); t[12 + c] = CM_B(t[12 + c], g[2 * c], R3s); }
#pragma unroll
    for (int h = 0; h < 4; ++h) { t[16 + 2 * h] = CM_B(t[16 + 2 * h], R[4 + h], g0s); t[17 + 2 * h] = CM_B(t[17 + 2 * h], R[4 + h], g4s); }
    SCHED_FENCE();
    *(unsigned*)(wb) = 0x00003c00u;
#pragma unroll
    for (int k = 1; k < 16; ++k) *(unsigned*)(wb + k * KG_RS) = pk16(Lo[k]);
#pragma unroll
    for (int c = 0; c < 8; ++c) *(unsigned*)(hb + c * KG_RS) = pk16(g[c]);
#pragma unroll
    for (int i = 0; i < 24; ++i) *(unsigned*)(hb + (8 + i) * KG_RS) = pk16(t[i]);
    SCHED_FENCE();
  }
  {
    const f32x2 z0s = cswz(z[0]);
    f32x2 t[9];
#pragma unroll
    for (int k = 1; k < 8; ++k) t[k] = (f32x2){R[k].x, R[k].x} * z[0];
    t[8] = (f32x2){R16.x, R16.x} * g[0];
    SCHED_FENCE();
#pragma unroll
    for (int k = 1; k < 8; ++k) z[k] = CM_B(t[k], R[k], z0s);
    z[8] = CM_B(t[8], R16, g0s);
    SCHED_FENCE();
#pragma unroll
    for (int h = 0; h < 9; ++h) *(unsigned*)(hb + (32 + h) * KG_RS) = pk16(z[h]);
  }
  STAMP(2);
  {
    const int r = lane & 15, gq = lane >> 4;
    const char* base = smem + kq * KG_WAVE + r * KG_RS + gq * 16;
    f32x4 acc[3] = {};
#pragma unroll
    for (int ks = 0; ks < 4; ++ks) {
      const f16x8 b = *(const f16x8*)(base + ks * 64);
#pragma unroll
      for (int m = 0; m < 3; ++m) {
        const f16x8 a = *(const f16x8*)(base + (16 + 16 * m) * KG_RS + ks * 64);
        acc[m] = __builtin_amdgcn_mfma_f32_16x16x32_f16(a, b, acc[m], 0, 0, 0);
      }
    }
    float* part = (float*)(smem + KG_PART) + kq;
#pragma unroll
    for (int m = 0; m < 3; ++m)
#pragma unroll
      for (int j = 0; j < 4; ++j) part[((16 * m + 4 * gq + j) * 16 + r) * 4] = acc[m][j] * winv;
  }
  STAMP(3);
  __syncthreads();
  STAMP(4);
  {
    float scale, inv_scale;
    pow2_scale((red[0] + red[1]) + (red[2] + red[3]), scale, inv_scale);
    const f32x4* part = (const f32x4*)(smem + KG_PART);
    unsigned pk[4];
#pragma unroll
    for (int j = 0; j < 4; ++j) {
      const f32x4 p0 = part[tab[j] & 0xffffu], p1 = part[tab[j] >> 16];
      pk[j] = pack_f16(((p0[0] + p0[1]) + (p0[2] + p0[3])) * scale, ((p1[0] + p1[1]) + (p1[2] + p1[3])) * scale);
    }
    const __amdgpu_buffer_rsrc_t rs = ws_rsrc(ws);
    const size_t kr = OFF_KR + (size_t)d * KR_LEN * 2;
    wst128(rs, kr + tid * 16, (u32x4){pk[0], pk[1], pk[2], pk[3]});
    if (tid < 32) wst128(rs, kr + 4096 + tid * 16, (u32x4){0u, 0u, 0u, 0u});
    if (tid == 0) ((float*)(ws + OFF_KS))[d] = inv_scale;
  }
  asm volatile("" ::"v"(pf));
  STAMP(5); STAMPW(14);
}

DI float reduce4(const float (&a)[4]) {
  float v[2];
#pragma unroll
  for (int i = 0; i < 2; ++i) {
    auto r = __builtin_amdgcn_permlane32_swap(__float_as_uint(a[i]), __float_as_uint(a[i + 2]), false, false);
    v[i] = __uint_as_float(r[0]) + __uint_as_float(r[1]);
  }
  auto r = __builtin_amdgcn_permlane16_swap(__float_as_uint(v[0]), __float_as_uint(v[1]), false, false);
  float x = __uint_as_float(r[0]) + __uint_as_float(r[1]);
  x = dpp_add<0xB1>(x);
  x = dpp_add<0x4E>(x);
  x = dpp_add<0x141>(x);
  x = dpp_add<0x140>(x);
  return x;
}
DI float bcast_row(float w, int t) { return __uint_as_float(__builtin_amdgcn_readlane(__float_as_uint(w), 16 * t)); }

constexpr int LN_PL = 448 * 64 + 56 * 16;
DI void k1_ln(char* smem, int blk, const u16* __restrict__ x, char* __restrict__ ws) {
  const int tid = threadIdx.x, lane = tid & 63, wv = tid >> 6;
  STAMP_BASE(ws, 0); STAMP(0);
  const int tok0 = blk * 32 + wv * 8;
  const bool act = lane < 56;
  u32x4 xr[8];
#pragma unroll
  for (int t = 0; t < 8; ++t) {
    xr[t] = (u32x4){0u, 0u, 0u, 0u};
    if (act) xr[t] = *(const u32x4*)(x + (size_t)(tok0 + t) * D_ + lane * 8);
  }
  f32x2 xf[8][4];
  float part[2][4], wm[2], wr[2], wb[2];
#pragma unroll
  for (int t = 0; t < 8; ++t) {
#pragma unroll
    for (int j = 0; j < 4; ++j) xf[t][j] = (f32x2){bflo(xr[t][j]), bfhi(xr[t][j])};
    const f32x2 s2 = (xf[t][0] + xf[t][1]) + (xf[t][2] + xf[t][3]);
    part[t >> 2][t & 3] = s2.x + s2.y;
  }
#pragma unroll
  for (int g = 0; g < 2; ++g) wm[g] = reduce4(part[g]) * (1.f / D_);
#pragma unroll
  for (int t = 0; t < 8; ++t) {
    const float m = bcast_row(wm[t >> 2], t & 3);
    f32x2 q2 = {0.f, 0.f};
#pragma unroll
    for (int j = 0; j < 4; ++j) { const f32x2 dd = xf[t][j] - (f32x2){m, m}; q2 = __builtin_elementwise_fma(dd, dd, q2); }
    part[t >> 2][t & 3] = act ? q2.x + q2.y : 0.f;
  }
#pragma unroll
  for (int g = 0; g < 2; ++g) {
    const float wq = reduce4(part[g]);
    const float sd = rbf(sqrtf(rbf(rbf(wq * (1.f / D_)) + 1e-5f)));
    wr[g] = 1.f / sd;
    wb[g] = rbf(wm[g]);
  }
  STAMP(1);
  f32x2 un[8][4];
#pragma unroll
  for (int t = 0; t < 8; ++t) {
    const float mb = bcast_row(wb[t >> 2], t & 3), ri = bcast_row(wr[t >> 2], t & 3);
#pragma unroll
    for (int j = 0; j < 4; ++j) {
      const f32x2 dd = xf[t][j] - (f32x2){mb, mb};
      const unsigned pb = pack_bf16(dd.x, dd.y);
      un[t][j] = (f32x2){bflo(pb), bfhi(pb)} * (f32x2){ri, ri};
    }
  }
  if (act) {
    char* ub = smem + lane * (8 * 64 + 16) + wv * 16;
#pragma unroll
    for (int j = 0; j < 8; ++j) {
      u32x4 pu, px;
#pragma unroll
      for (int t = 0; t < 4; ++t) {
        pu[t] = pack_bf16(un[2 * t][j >> 1][j & 1], un[2 * t + 1][j >> 1][j & 1]);
        const unsigned a = xr[2 * t][j >> 1], b = xr[2 * t + 1][j >> 1];
        px[t] = (j & 1) ? __builtin_amdgcn_perm(b, a, 0x07060302u) : __builtin_amdgcn_perm(b, a, 0x05040100u);
      }
      *(u32x4*)(ub + j * 64) = pu;
      *(u32x4*)(ub + LN_PL + j * 64) = px;
    }
  }
  STAMP(2);
  __syncthreads();
  STAMP(3);
  const __amdgpu_buffer_rsrc_t wrs = ws_rsrc(ws);
#pragma unroll
  for (int i = 0; i < 7; ++i) {
    const int ch = i * 64 + (tid >> 2), v = tid & 3;
    const int o = ch * 64 + (ch >> 3) * 16 + v * 16;
    const u32x4 a = *(const u32x4*)(smem + o);
    const u32x4 b = *(const u32x4*)(smem + LN_PL + o);
    __builtin_amdgcn_raw_buffer_store_b128(a, wrs, (unsigned)(OFF_UT + ((size_t)ch * L_ + blk * 32 + v * 8) * 2), 0, LN_AUX);
    __builtin_amdgcn_raw_buffer_store_b128(b, wrs, (unsigned)(OFF_XT + ((size_t)ch * L_ + blk * 32 + v * 8) * 2), 0, LN_AUX);
  }
  STAMP(4); STAMPW(14);
}

__global__ __launch_bounds__(256, 2) void k1_kernel(const u16* __restrict__ x, const u16* __restrict__ lar,
                                                    const u16* __restrict__ aim, const u16* __restrict__ cr,
                                                    const u16* __restrict__ ci, const u16* __restrict__ ldt,
                                                    const u16* __restrict__ w1, const u16* __restrict__ w2,
                                                    char* __restrict__ ws) {
  __shared__ __attribute__((aligned(16))) char smem[K1_LDS];
  int b = blockIdx.x;
  if (b < K1_LN) {
    if (b == 0 && threadIdx.x < 32) ((unsigned*)(ws + OFF_CNT))[threadIdx.x * 64] = 0u;
    k1_ln(smem, b, x, ws);
    return;
  }
  k1_kgen(smem, b - K1_LN, lar, aim, cr, ci, ldt, w1, w2, ws);
}

DI void wt_store(const u16* t, __amdgpu_buffer_rsrc_t rs, size_t dsto, int tid) {
#pragma unroll
  for (int i = 0; i < 4; ++i) {
    const int c = i * 512 + tid, ks = c >> 6, l = c & 63;
    if (c < 1792) {
      const int col = perm23(l & 31), k0 = ks * 16 + (l >> 5) * 8;
      unsigned w[4];
#pragma unroll
      for (int j = 0; j < 4; ++j)
        w[j] = (unsigned)t[(k0 + 2 * j) * 36 + col] | ((unsigned)t[(k0 + 2 * j + 1) * 36 + col] << 16);
      wst128(rs, dsto + (size_t)c * 16, (u32x4){w[0], w[1], w[2], w[3]});
    }
  }
}
DI void wt_fill(u16* t, const u32x4 (&v)[4], int tid) {
#pragma unroll
  for (int i = 0; i < 4; ++i) {
    const int c = i * 512 + tid, k = c >> 2, q = c & 3;
    if (c < 1792) {
      *(u32x2*)(t + k * 36 + q * 8) = (u32x2){v[i][0], v[i][1]};
      *(u32x2*)(t + k * 36 + q * 8 + 4) = (u32x2){v[i][2], v[i][3]};
    }
  }
}
DI void k2_wtile(char* smem, int b, const u16* __restrict__ w1, const u16* __restrict__ b1, const u16* __restrict__ w2, char* ws) {
  const int tid = threadIdx.x;
  const __amdgpu_buffer_rsrc_t rs = ws_rsrc(ws);
  STAMP_BASE(ws, 1); STAMP(0);
  u16* t = (u16*)smem;
  const int dt = b >> 2, kc = b & 3;
  const u16* s1 = w1 + b * 32;
  const u16* s2 = w2 + (size_t)kc * 448 * D_ + dt * 32;
  u32x4 v1[4], v2[4];
#pragma unroll
  for (int i = 0; i < 4; ++i) {
    const int c = i * 512 + tid, k = c >> 2, q = c & 3;
    if (c < 1792) { v1[i] = *(const u32x4*)(s1 + (size_t)k * F_ + q * 8); v2[i] = *(const u32x4*)(s2 + (size_t)k * D_ + q * 8); }
  }
  const unsigned b1v = b1[b * 32 + perm23(tid & 31)];
  SCHED_FENCE();
  wt_fill(t, v1, tid);
  __syncthreads();
  STAMP(1);
  const size_t d1 = OFF_W1 + (size_t)b * W1_FRAGS * 1024;
  wt_store(t, rs, d1, tid);
  float* red = (float*)(smem + 448 * 72);
  {
    const int col = tid & 31, kg = tid >> 5;
    float s = 0.f;
#pragma unroll
    for (int k = 0; k < 28; ++k) s += bf2f(t[(kg * 28 + k) * 36 + col]);
    red[tid] = s;
  }
  __syncthreads();
  STAMP(2);
  if (tid < 64) {
    const int c = perm23(tid & 31);
    float a = 0.f;
#pragma unroll
    for (int g = 0; g < 16; ++g) a += red[g * 32 + c];
    const float hi = rbf(a);
    const unsigned w0 = pack_bf16(hi, a - hi);
    wst128(rs, d1 + (size_t)(28 * 64 + tid) * 16, (u32x4){tid < 32 ? w0 : 0u, tid < 32 ? b1v : 0u, 0u, 0u});
  }
  wt_fill(t, v2, tid);
  __syncthreads();
  wt_store(t, rs, OFF_W2 + ((size_t)dt * 112 + kc * 28) * 1024, tid);
  STAMP(3); STAMPW(14);
}

constexpr int CV_CS = 4160;
constexpr int CV_KOFF = 8 * CV_CS;
constexpr int CV_LDS = 8 * 7 * 1024;
static_assert(CV_LDS >= CV_KOFF + KR_LEN * 2, "lds");
constexpr int K2_GRID = D_ + 56;

__global__ __launch_bounds__(512, 4) void k2_kernel(const u16* __restrict__ dskip, const u16* __restrict__ w1,
                                                 const u16* __restrict__ b1, const u16* __restrict__ w2,
                                                 char* __restrict__ ws) {
  __shared__ __attribute__((aligned(16))) char smem[CV_LDS];
  if (blockIdx.x < 56) {
    k2_wtile(smem, blockIdx.x, w1, b1, w2, ws);
    return;
  }
  const int d = blockIdx.x - 56, tid = threadIdx.x, lane = tid & 63;
  const int wv = __builtin_amdgcn_readfirstlane(tid >> 6);
  STAMP_BASE(ws, 1); STAMP(0);
  const u16* uT = (const u16*)(ws + OFF_UT) + (size_t)d * L_;
  const u16* xT = (const u16*)(ws + OFF_XT) + (size_t)d * L_;
  const char* kr = ws + OFF_KR + (size_t)d * KR_LEN * 2;
  const int m0 = tid & 255, half = wv >> 2;
  u32x4 c0 = {0u, 0u, 0u, 0u};
  if (m0 > 0) c0 = *(const u32x4*)(uT + (m0 - 1) * 8);
  const u32x4 c1 = *(const u32x4*)(uT + m0 * 8);
  u32x4 k0 = {0u, 0u, 0u, 0u};
  if (tid < 288) k0 = *(const u32x4*)(kr + tid * 16);
  const int c16 = lane & 15, g4 = lane >> 4;
  const int pe = 256 * wv + 16 * c16 + 4 * g4;
  const u32x2 ue = *(const u32x2*)(uT + pe), xe = *(const u32x2*)(xT + pe);
  const float inv_scale = ((const float*)(ws + OFF_KS))[d];
  const float dsk = bf2f(dskip[d]);
  SCHED_FENCE();
  STAMP(1);
  if (tid < 288) *(u32x4*)(smem + CV_KOFF + tid * 16) = k0;
  if (tid < 8) *(u32x4*)(smem + tid * CV_CS) = (u32x4){0u, 0u, 0u, 0u};
  {
    unsigned p[8];
#pragma unroll
    for (int j = 0; j < 4; ++j) {
      p[j] = pack_f16(bflo(c0[j]), bfhi(c0[j]));
      p[4 + j] = pack_f16(bflo(c1[j]), bfhi(c1[j]));
    }
    char* dst = smem + (m0 + 1) * 16 + half * 4 * CV_CS;
    if (half == 0) {
      unsigned q[5];
#pragma unroll
      for (int i = 0; i < 5; ++i) q[i] = __builtin_amdgcn_alignbit(p[i + 1], p[i], 16);
      *(u32x4*)(dst) = (u32x4){q[0], q[1], q[2], q[3]};
      *(u32x4*)(dst + CV_CS) = (u32x4){p[1], p[2], p[3], p[4]};
      *(u32x4*)(dst + 2 * CV_CS) = (u32x4){q[1], q[2], q[3], q[4]};
      *(u32x4*)(dst + 3 * CV_CS) = (u32x4){p[2], p[3], p[4], p[5]};
    } else {
      unsigned q[5];
#pragma unroll
      for (int i = 0; i < 5; ++i) q[i] = __builtin_amdgcn_alignbit(p[i + 3], p[i + 2], 16);
      *(u32x4*)(dst) = (u32x4){q[0], q[1], q[2], q[3]};
      *(u32x4*)(dst + CV_CS) = (u32x4){p[3], p[4], p[5], p[6]};
      *(u32x4*)(dst + 2 * CV_CS) = (u32x4){q[1], q[2], q[3], q[4]};
      *(u32x4*)(dst + 3 * CV_CS) = (u32x4){p[4], p[5], p[6], p[7]};
    }
  }
  STAMP(2);
  __syncthreads();
  STAMP(3);
  f32x4 acc[8] = {};
  {
    const char* abase = smem + (c16 & 7) * CV_CS + ((c16 & 8) + 8 * g4) * 2 + wv * 64;
    const char* bbase = smem + CV_KOFF + (2032 - 16 * c16 + 8 * g4) * 2 + wv * 64;
    f16x8 A[8], B[8];
#pragma unroll
    for (int i = 0; i < 8; ++i) {
      A[i] = *(const f16x8*)(abase + i * 512);
      B[i] = *(const f16x8*)(bbase - i * 512);
    }
#pragma unroll
    for (int t = 0; t < 8; ++t)
#pragma unroll
      for (int ia = 0; ia <= t; ++ia)
        acc[t] = __builtin_amdgcn_mfma_f32_16x16x32_f16(A[ia], B[t - ia], acc[t], 0, 0, 0);
  }
  STAMP(4);
  __syncthreads();
  STAMP(5);
  f32x4* red = (f32x4*)smem;
  f32x4 own = acc[0];
#pragma unroll
  for (int t = 0; t < 8; ++t) {
    if (t == wv) own = acc[t];
    else red[(t * 7 + (wv > t ? wv - 1 : wv)) * 64 + lane] = acc[t];
  }
  __syncthreads();
  {
    f32x4 s[7];
#pragma unroll
    for (int i = 0; i < 7; ++i) s[i] = red[(wv * 7 + i) * 64 + lane];
    const f32x4 sum = ((own + s[0]) + (s[1] + s[2])) + ((s[3] + s[4]) + (s[5] + s[6]));
    float h[4];
#pragma unroll
    for (int j = 0; j < 4; ++j) {
      const float uu = (j & 1) ? bfhi(ue[j >> 1]) : bflo(ue[j >> 1]);
      const float xx = (j & 1) ? bfhi(xe[j >> 1]) : bflo(xe[j >> 1]);
      const float y = rbf(sum[j] * inv_scale);
      const float tt = rbf(y + rbf(uu * dsk));
      h[j] = xx + tt;
    }
    const __amdgpu_buffer_rsrc_t rs = ws_rsrc(ws);
    wst64(rs, OFF_HT + ((size_t)d * HT_LD + pe) * 2, (u32x2){pack_bf16(h[0], h[1]), pack_bf16(h[2], h[3])});
  }
  STAMP(6); STAMPW(14);
}

constexpr int K3_TILE = 448 * 128;
constexpr int K3_RED = 8 * 7 * 2048;
constexpr int K3_STAT = K3_RED;
constexpr int K3_LDS = K3_STAT + 4 * 64 * 8;
typedef __attribute__((address_space(3))) bf16x4* lds_bf16x4_p;
typedef __attribute__((address_space(1))) unsigned gu32;

DI bf16x8 k3_bfrag(const char* smem, unsigned lane_off0, unsigned lane_off1, int off) {
  const bf16x4 lo = __builtin_amdgcn_ds_read_tr16_b64_v4bf16((lds_bf16x4_p)(smem + lane_off0 + off));
  const bf16x4 hi = __builtin_amdgcn_ds_read_tr16_b64_v4bf16((lds_bf16x4_p)(smem + lane_off1 + off));
  return __builtin_shufflevector(lo, hi, 0, 1, 2, 3, 4, 5, 6, 7);
}
#define K34_WIN 3
#define W2_A 4
#define H1_AUX 16
#define H1_WIN 6
template <int N> DI void wait_vm() { asm volatile("s_waitcnt vmcnt(%0)" ::"i"(N) : "memory"); }

__global__ __launch_bounds__(512, 2) void k34_kernel(const u16* __restrict__ b2, char* __restrict__ ws,
                                                     u16* __restrict__ out) {
  __shared__ __attribute__((aligned(16))) char smem[K3_LDS];
  const int tid = threadIdx.x, lane = tid & 63;
  const int wv = __builtin_amdgcn_readfirstlane(tid >> 6);
  const int xcd = blockIdx.x & 7, idx = blockIdx.x >> 3;
  const int grp = 4 * xcd + idx / 7, j = idx % 7;
  const int tok0 = grp * 64, ft = j * 8 + wv;
  const int sh = (wv >> 1) & 1;
  const int s2 = wv >> 2;
  STAMP_BASE(ws, 2); STAMP(0);
  const u16* hsrc = (const u16*)(ws + OFF_HT) + (size_t)(tid >> 3) * HT_LD + tok0 + (tid & 7) * 8;
  const bf16x8* w1f = (const bf16x8*)(ws + OFF_W1) + (size_t)ft * W1_FRAGS * 64 + lane;
  const bf16x8* w1a = w1f + sh * 128;
  const bf16x8* w1b = w1f + (1 - sh) * 128;
  const __amdgpu_buffer_rsrc_t w2rs = __builtin_amdgcn_make_buffer_rsrc((void*)(ws + OFF_W2), 0, D_ * F_ * 2, 0x00020000);
  const unsigned w2v = ((2 * j) * 112 * 64 + lane) * 16;
#define KSLOT(sl) ((sl) < 2 ? 16 * j + 2 * wv + (sl) : (12 * wv + (sl) - 2) + ((12 * wv + (sl) - 2) >= 16 * j ? 16 : 0))
#define LDW2(sl, dt) __builtin_bit_cast(bf16x8, __builtin_amdgcn_raw_buffer_load_b128(w2rs, w2v + (dt) * 112 * 1024, KSLOT(sl) * 1024, 0))
  u32x4 st[7];
  bf16x8 A[28];
  bf16x8 a0[14], a1[14];
#define ISSUE1(c) do { st[c] = *(const u32x4*)(hsrc + (size_t)(64 * (c)) * HT_LD); \
    A[4 * (c)] = w1a[(4 * (c)) * 64]; A[4 * (c) + 1] = w1a[(4 * (c) + 1) * 64]; \
    A[4 * (c) + 2] = w1b[(4 * (c)) * 64]; A[4 * (c) + 3] = w1b[(4 * (c) + 1) * 64]; } while (0)
#define ISSUE2(c) do { a0[2 * (c)] = LDW2(2 * (c), 0); a1[2 * (c)] = LDW2(2 * (c), 1); \
    a0[2 * (c) + 1] = LDW2(2 * (c) + 1, 0); a1[2 * (c) + 1] = LDW2(2 * (c) + 1, 1); } while (0)
#pragma unroll
  for (int c = 0; c < K34_WIN; ++c) { ISSUE1(c); SCHED_FENCE(); }
  const bf16x8 Aext = w1f[28 * 64];
  SCHED_FENCE();
  const int hh = lane >> 5, col = lane & 31;
  const unsigned q = (lane & 15) >> 2, p = lane & 3, blk = (lane >> 4) & 1;
  char* lw = smem + 128 * (tid >> 3) + 16 * ((tid & 7) ^ (((tid >> 4) & 1) << 2));
  unsigned oa[2][2], ob[2][2];
  {
    const unsigned r0 = 8 * hh + q, r1 = 8 * hh + 4 + q, sw = ((q >> 1) & 1) << 2;
#pragma unroll
    for (int it = 0; it < 2; ++it) {
      const unsigned c = (wv & 1) ^ it;
      const unsigned chk = (4 * c + 2 * blk + (p >> 1)) ^ sw;
      const unsigned x0 = 128 * r0 + 16 * chk + 8 * (p & 1), x1 = 128 * r1 + 16 * chk + 8 * (p & 1);
      oa[it][0] = x0 + sh * 4096; oa[it][1] = x1 + sh * 4096;
      ob[it][0] = x0 + (1 - sh) * 4096; ob[it][1] = x1 + (1 - sh) * 4096;
    }
  }
  f32x16 acc[2] = {};
  float ssum = 0.f, ssq = 0.f;
  const bf16x2 ones2 = {(__bf16)1.0f, (__bf16)1.0f};
#define WRITE1(c) do { *(u32x4*)(lw + (c) * 8192) = st[c]; } while (0)
#define RDB(n, it) ((((n) & 2) == 0) ? k3_bfrag(smem, oa[it][0], oa[it][1], ((n) >> 2) * 8192 + ((n) & 1) * 2048) \
                                      : k3_bfrag(smem, ob[it][0], ob[it][1], ((n) >> 2) * 8192 + ((n) & 1) * 2048))
  bf16x8 Bf[3][2];
  WRITE1(0);
  WRITE1(1);
  __syncthreads();
  STAMP(1);
  Bf[0][0] = RDB(0, 0); Bf[0][1] = RDB(0, 1); Bf[1][0] = RDB(1, 0); Bf[1][1] = RDB(1, 1);
#pragma unroll
  for (int n = 0; n < 28; ++n) {
    const int c = n >> 2, i = n & 3;
    acc[0] = __builtin_amdgcn_mfma_f32_32x32x16_bf16(A[n], Bf[n % 3][0], acc[0], 0, 0, 0);
    if (n + 2 < 28) Bf[(n + 2) % 3][0] = RDB(n + 2, 0);
    if (i < 2) {
      const u32x4 B = __builtin_bit_cast(u32x4, Bf[n % 3][0]);
      const bf16x2 pr = __builtin_bit_cast(bf16x2, s2 ? B[2] : B[0]);
      ssum = __builtin_amdgcn_fdot2_f32_bf16(pr, ones2, ssum, false);
      ssq = __builtin_amdgcn_fdot2_f32_bf16(pr, pr, ssq, false);
    }
    SCHED_FENCE();
    acc[1] = __builtin_amdgcn_mfma_f32_32x32x16_bf16(A[n], Bf[n % 3][1], acc[1], 0, 0, 0);
    if (n + 2 < 28) Bf[(n + 2) % 3][1] = RDB(n + 2, 1);
    if (i < 2) {
      const u32x4 B = __builtin_bit_cast(u32x4, Bf[n % 3][0]);
      const bf16x2 pr = __builtin_bit_cast(bf16x2, s2 ? B[3] : B[1]);
      ssum = __builtin_amdgcn_fdot2_f32_bf16(pr, ones2, ssum, false);
      ssq = __builtin_amdgcn_fdot2_f32_bf16(pr, pr, ssq, false);
    }
    if (i == 0 && c + 2 < 7) WRITE1(c + 2);
    if (i == 2) { if (c + K34_WIN < 7) ISSUE1(c + K34_WIN); else if (c + K34_WIN - 7 < W2_A) ISSUE2(c + K34_WIN - 7); }
    if (n == 27) { _Pragma("unroll") for (int z_ = K34_WIN; z_ < W2_A; ++z_) ISSUE2(z_); }
    SCHED_FENCE();
    if (i == 3 && c + 2 < 7) __syncthreads();
  }
  STAMP(2);
  float2* stats = (float2*)(smem + K3_STAT);
  {
    auto r1 = __builtin_amdgcn_permlane32_swap(__float_as_uint(ssum), __float_as_uint(ssum), false, false);
    ssum = __uint_as_float(r1[0]) + __uint_as_float(r1[1]);
    auto r2 = __builtin_amdgcn_permlane32_swap(__float_as_uint(ssq), __float_as_uint(ssq), false, false);
    ssq = __uint_as_float(r2[0]) + __uint_as_float(r2[1]);
    if (hh == 0) stats[(wv >> 1) * 64 + (wv & 1) * 32 + col] = make_float2(ssum, ssq);
  }
  const int t2 = wv >> 1, hf2 = wv & 1;
  const int c2 = t2 & 1;
  bf16x8 res;
  {
    const unsigned r0 = 8 * hh + q, r1 = 8 * hh + 4 + q, sw = ((q >> 1) & 1) << 2;
    const unsigned chk = (4 * c2 + 2 * blk + (p >> 1)) ^ sw;
    res = k3_bfrag(smem, 128 * r0 + 16 * chk + 8 * (p & 1), 128 * r1 + 16 * chk + 8 * (p & 1), (j * 4 + (t2 >> 1) * 2 + hf2) * 2048);
  }
  __syncthreads();
  float sdv[2], c1v[2], c2v[2];
#pragma unroll
  for (int it = 0; it < 2; ++it) {
    const unsigned c = (wv & 1) ^ it;
    const float2 s0 = stats[c * 32 + col], s1 = stats[64 + c * 32 + col], s2_ = stats[128 + c * 32 + col], s3 = stats[192 + c * 32 + col];
    const float mean = ((s0.x + s1.x) + (s2_.x + s3.x)) * (1.f / D_);
    const float var = fmaxf(__builtin_fmaf(-mean, mean, ((s0.y + s1.y) + (s2_.y + s3.y)) * (1.f / D_)), 0.f);
    const float sd = rbf(sqrtf(rbf(rbf(var) + 1e-5f)));
    const float rs = __builtin_amdgcn_rcpf(sd);
    sdv[it] = sd;
    c1v[it] = rs * -2.3022082f;
    c2v[it] = rs * rs * rs * -0.10294324f;
    const unsigned w0 = pack_bf16(-mean, -mean), w1 = pack_bf16(sd, 0.f);
    const u32x4 bw = {hh ? 0u : w0, hh ? 0u : w1, 0u, 0u};
    acc[it] = __builtin_amdgcn_mfma_f32_32x32x16_bf16(Aext, __builtin_bit_cast(bf16x8, bw), acc[it], 0, 0, 0);
  }
  const __amdgpu_buffer_rsrc_t h1rs = __builtin_amdgcn_make_buffer_rsrc((void*)(ws + OFF_H1), 0, L_ * F_ * 2, 0x00020000);
  u32x4 own[2][2];
#pragma unroll
  for (int it = 0; it < 2; ++it) {
    const unsigned c = (wv & 1) ^ it;
    const unsigned fb = (((grp * 2 + c) * 112 + 2 * ft) * 64 + lane) * 16;
    const f32x2 c1p = {c1v[it], c1v[it]}, c2p = {c2v[it], c2v[it]}, sdp = {sdv[it], sdv[it]};
#pragma unroll
    for (int hf = 0; hf < 2; ++hf) {
      f32x2 a[4], z[4];
#pragma unroll
      for (int e = 0; e < 4; ++e) a[e] = (f32x2){acc[it][8 * hf + 2 * e], acc[it][8 * hf + 2 * e + 1]};
#pragma unroll
      for (int e = 0; e < 4; ++e) z[e] = a[e] * a[e];
#pragma unroll
      for (int e = 0; e < 4; ++e) z[e] = __builtin_elementwise_fma(z[e], c2p, c1p);
#pragma unroll
      for (int e = 0; e < 4; ++e) z[e] = a[e] * z[e];
#pragma unroll
      for (int e = 0; e < 4; ++e) z[e] = (f32x2){__builtin_amdgcn_exp2f(z[e].x), __builtin_amdgcn_exp2f(z[e].y)};
#pragma unroll
      for (int e = 0; e < 4; ++e) z[e] = __builtin_elementwise_fma(z[e], sdp, sdp);
#pragma unroll
      for (int e = 0; e < 4; ++e) z[e] = (f32x2){__builtin_amdgcn_rcpf(z[e].x), __builtin_amdgcn_rcpf(z[e].y)};
#pragma unroll
      for (int e = 0; e < 4; ++e) z[e] = a[e] * z[e];
      const u32x4 pk = {pack_bf16(z[0].x, z[0].y), pack_bf16(z[1].x, z[1].y), pack_bf16(z[2].x, z[2].y), pack_bf16(z[3].x, z[3].y)};
      __builtin_amdgcn_raw_buffer_store_b128(pk, h1rs, fb + hf * 1024, 0, 16);
      own[it][hf] = pk;
      SCHED_FENCE();
    }
  }
  f32x16 acc2[4] = {};
#pragma unroll
  for (int ks = 0; ks < 2; ++ks) {
    const bf16x8 y0 = __builtin_bit_cast(bf16x8, own[0][ks]), y1 = __builtin_bit_cast(bf16x8, own[1][ks]);
    acc2[0] = __builtin_amdgcn_mfma_f32_32x32x16_bf16(a0[ks], y0, acc2[0], 0, 0, 0);
    acc2[1] = __builtin_amdgcn_mfma_f32_32x32x16_bf16(a0[ks], y1, acc2[1], 0, 0, 0);
    acc2[2] = __builtin_amdgcn_mfma_f32_32x32x16_bf16(a1[ks], y0, acc2[2], 0, 0, 0);
    acc2[3] = __builtin_amdgcn_mfma_f32_32x32x16_bf16(a1[ks], y1, acc2[3], 0, 0, 0);
  }
  STAMP(3);
  wait_vm<0>();
  __builtin_amdgcn_s_barrier();
  asm volatile("" ::: "memory");
  STAMP(4);
  gu32* cnt = (gu32*)(ws + OFF_CNT) + grp * 64;
  if (wv == 0) {
    if (lane == 0) {
      __hip_atomic_fetch_add(cnt, 1u, __ATOMIC_RELAXED, __HIP_MEMORY_SCOPE_AGENT);
      for (unsigned spins = 0; spins < (1u << 20); ++spins) {
        if (__hip_atomic_load(cnt, __ATOMIC_RELAXED, __HIP_MEMORY_SCOPE_AGENT) >= 7u) break;
        __builtin_amdgcn_s_sleep(1);
      }
    }
    __builtin_amdgcn_fence(__ATOMIC_ACQUIRE, "wavefront");
  }
  SCHED_FENCE();
#pragma unroll
  for (int z_ = W2_A; z_ < 7; ++z_) ISSUE2(z_);
  SCHED_FENCE();
  __builtin_amdgcn_s_barrier();
  __builtin_amdgcn_fence(__ATOMIC_ACQUIRE, "wavefront");
  asm volatile("" ::: "memory");
  STAMP(5);
  const unsigned hb0 = (((grp * 2 + (wv & 1)) * 112) * 64 + lane) * 16, hb1 = (((grp * 2 + 1 - (wv & 1)) * 112) * 64 + lane) * 16;
  u32x4 b0[H1_WIN], b1v[H1_WIN];
#pragma unroll
  for (int i = 0; i < H1_WIN; ++i) {
    b0[i] = __builtin_amdgcn_raw_buffer_load_b128(h1rs, hb0, KSLOT(2 + i) * 1024, H1_AUX);
    b1v[i] = __builtin_amdgcn_raw_buffer_load_b128(h1rs, hb1, KSLOT(2 + i) * 1024, H1_AUX);
  }
  const int d0 = j * 64 + (t2 >> 1) * 32 + 16 * hf2 + 8 * hh;
  const u32x4 bz = *(const u32x4*)(b2 + d0);
  SCHED_FENCE();
#pragma unroll
  for (int ks = 0; ks < 12; ++ks) {
    const int sl = ks % H1_WIN;
    const bf16x8 y0 = __builtin_bit_cast(bf16x8, b0[sl]), y1 = __builtin_bit_cast(bf16x8, b1v[sl]);
    acc2[0] = __builtin_amdgcn_mfma_f32_32x32x16_bf16(a0[2 + ks], y0, acc2[0], 0, 0, 0);
    acc2[1] = __builtin_amdgcn_mfma_f32_32x32x16_bf16(a0[2 + ks], y1, acc2[1], 0, 0, 0);
    acc2[2] = __builtin_amdgcn_mfma_f32_32x32x16_bf16(a1[2 + ks], y0, acc2[2], 0, 0, 0);
    acc2[3] = __builtin_amdgcn_mfma_f32_32x32x16_bf16(a1[2 + ks], y1, acc2[3], 0, 0, 0);
    if (ks + H1_WIN < 12) {
      b0[sl] = __builtin_amdgcn_raw_buffer_load_b128(h1rs, hb0, KSLOT(2 + ks + H1_WIN) * 1024, H1_AUX);
      b1v[sl] = __builtin_amdgcn_raw_buffer_load_b128(h1rs, hb1, KSLOT(2 + ks + H1_WIN) * 1024, H1_AUX);
    }
    SCHED_FENCE();
  }
  STAMP(6);
  f32x4* red = (f32x4*)smem;
  f32x4 own0 = {}, own1 = {};
#pragma unroll
  for (int a = 0; a < 8; ++a) {
    const int u = (a ^ (2 * (wv & 1)));
    const f32x4 v0 = {acc2[a >> 1][8 * (a & 1)], acc2[a >> 1][8 * (a & 1) + 1], acc2[a >> 1][8 * (a & 1) + 2], acc2[a >> 1][8 * (a & 1) + 3]};
    const f32x4 v1 = {acc2[a >> 1][8 * (a & 1) + 4], acc2[a >> 1][8 * (a & 1) + 5], acc2[a >> 1][8 * (a & 1) + 6], acc2[a >> 1][8 * (a & 1) + 7]};
    if (u == wv) { own0 = v0; own1 = v1; }
    else {
      const int slot = u * 7 + (wv > u ? wv - 1 : wv);
      red[(slot * 2) * 64 + lane] = v0;
      red[(slot * 2 + 1) * 64 + lane] = v1;
    }
  }
  __syncthreads();
  STAMP(7);
  {
    f32x4 s0[7], s1[7];
#pragma unroll
    for (int i = 0; i < 7; ++i) { s0[i] = red[((wv * 7 + i) * 2) * 64 + lane]; s1[i] = red[((wv * 7 + i) * 2 + 1) * 64 + lane]; }
    const f32x4 o0 = ((own0 + s0[0]) + (s0[1] + s0[2])) + ((s0[3] + s0[4]) + (s0[5] + s0[6]));
    const f32x4 o1 = ((own1 + s1[0]) + (s1[1] + s1[2])) + ((s1[3] + s1[4]) + (s1[5] + s1[6]));
    const u32x4 hr = __builtin_bit_cast(u32x4, res);
    unsigned pk[4];
#pragma unroll
    for (int e = 0; e < 4; ++e) {
      const float x0 = (e < 2) ? o0[2 * e] : o1[2 * e - 4], x1 = (e < 2) ? o0[2 * e + 1] : o1[2 * e - 3];
      const unsigned ob_ = pack_bf16(x0, x1);
      const unsigned o2 = pack_bf16(bflo(ob_) + bflo(bz[e]), bfhi(ob_) + bfhi(bz[e]));
      pk[e] = pack_bf16(bflo(hr[e]) + bflo(o2), bfhi(hr[e]) + bfhi(o2));
    }
    *(u32x4*)(out + (size_t)(tok0 + c2 * 32 + col) * D_ + d0) = (u32x4){pk[0], pk[1], pk[2], pk[3]};
  }
  STAMP(8); STAMPW(14);
}

extern "C" void kernel_launch(void* const* d_in, const int* in_sizes, int n_in, void* d_out, int out_size, void* d_ws,
                              size_t ws_size, hipStream_t stream) {
  const u16* x = (const u16*)d_in[0];
  const u16* lar = (const u16*)d_in[1];
  const u16* aim = (const u16*)d_in[2];
  const u16* cr = (const u16*)d_in[3];
  const u16* ci = (const u16*)d_in[4];
  const u16* ldt = (const u16*)d_in[5];
  const u16* dsk = (const u16*)d_in[6];
  const u16* w1 = (const u16*)d_in[8];
  const u16* b1 = (const u16*)d_in[9];
  const u16* w2 = (const u16*)d_in[10];
  const u16* b2 = (const u16*)d_in[11];
  char* ws = (char*)d_ws;
  k1_kernel<<<K1_GRID, 256, 0, stream>>>(x, lar, aim, cr, ci, ldt, w1, w2, ws);
  k2_kernel<<<K2_GRID, 512, 0, stream>>>(dsk, w1, b1, w2, ws);
  k34_kernel<<<224, 512, 0, stream>>>(b2, ws, (u16*)d_out);
}
```

```cpp
#include <hip/hip_runtime.h>
#include <hip/hip_bf16.h>
#include <stdint.h>

typedef __attribute__((ext_vector_type(8))) __bf16 bf16x8;
typedef __attribute__((ext_vector_type(4))) __bf16 bf16x4;
typedef __attribute__((ext_vector_type(2))) __bf16 bf16x2;
typedef __attribute__((ext_vector_type(8))) _Float16 f16x8;
typedef __attribute__((ext_vector_type(2))) _Float16 f16x2;
typedef __attribute__((ext_vector_type(2))) float f32x2;
typedef __attribute__((ext_vector_type(4))) float f32x4;
typedef __attribute__((ext_vector_type(16))) float f32x16;
typedef __attribute__((ext_vector_type(4))) unsigned u32x4;
typedef __attribute__((ext_vector_type(2))) unsigned u32x2;
typedef unsigned short u16;

#define DI __device__ __forceinline__
#define SCHED_FENCE() __builtin_amdgcn_sched_barrier(0)
#define STAMP_BASE(ws, kid)
#define STAMP(i)
#define STAMPW(i)

constexpr int L_ = 2048, D_ = 448, N_ = 256, F_ = 1792;
constexpr int KR_LEN = 2304;

constexpr size_t OFF_UT = 0;
constexpr size_t OFF_XT = OFF_UT + (size_t)D_ * L_ * 2;
constexpr size_t OFF_KR = OFF_XT + (size_t)D_ * L_ * 2;
constexpr size_t OFF_KS = OFF_KR + (size_t)D_ * KR_LEN * 2;
constexpr int W1_FRAGS = 29;
constexpr size_t OFF_CNT = OFF_KS + 2048;
constexpr size_t OFF_W1 = OFF_CNT + 32768;
constexpr size_t OFF_W2 = OFF_W1 + (size_t)56 * W1_FRAGS * 1024;
constexpr int HT_LD = L_ + 128;
constexpr size_t OFF_HT = OFF_W2 + (size_t)D_ * F_ * 2;
constexpr size_t OFF_H1 = OFF_HT + (size_t)D_ * HT_LD * 2;
constexpr size_t WS_END = OFF_H1 + (size_t)L_ * F_ * 2;

DI float bf2f(u16 v) { return __uint_as_float(((unsigned)v) << 16); }
DI float bflo(unsigned p) { return __uint_as_float(p << 16); }
DI float bfhi(unsigned p) { return __uint_as_float(p & 0xffff0000u); }
DI unsigned pack_bf16(float a, float b) {
  f32x2 v = {a, b};
  return __builtin_bit_cast(unsigned, __builtin_convertvector(v, bf16x2));
}
DI unsigned pack_f16(float a, float b) {
  f32x2 v = {a, b};
  return __builtin_bit_cast(unsigned, __builtin_convertvector(v, f16x2));
}
DI float rbf(float a) { return bflo(pack_bf16(a, a)); }
template <int CTRL> DI float dpp_add(float v) {
  const int t = __builtin_amdgcn_update_dpp(0, __float_as_int(v), CTRL, 0xf, 0xf, true);
  return v + __int_as_float(t);
}
DI float wave_sum(float v) {
  v = dpp_add<0xB1>(v);
  v = dpp_add<0x4E>(v);
  v = dpp_add<0x141>(v);
  v = dpp_add<0x140>(v);
  auto r = __builtin_amdgcn_permlane16_swap(__float_as_uint(v), __float_as_uint(v), false, false);
  v = __uint_as_float(r[0]) + __uint_as_float(r[1]);
  auto r2 = __builtin_amdgcn_permlane32_swap(__float_as_uint(v), __float_as_uint(v), false, false);
  return __uint_as_float(r2[0]) + __uint_as_float(r2[1]);
}
#define LN_AUX 2
#define WT_AUX 16
DI __amdgpu_buffer_rsrc_t ws_rsrc(char* ws) { return __builtin_amdgcn_make_buffer_rsrc((void*)ws, 0, (int)WS_END, 0x00020000); }
#define KR_AUX 16
#define HT_AUX 16
#define OUT_AUX 0
DI void wst128(__amdgpu_buffer_rsrc_t rs, size_t off, u32x4 v) { __builtin_amdgcn_raw_buffer_store_b128(v, rs, (unsigned)off, 0, WT_AUX); }
DI void wst128k(__amdgpu_buffer_rsrc_t rs, size_t off, u32x4 v) { __builtin_amdgcn_raw_buffer_store_b128(v, rs, (unsigned)off, 0, KR_AUX); }
DI void wst64(__amdgpu_buffer_rsrc_t rs, size_t off, u32x2 v) { __builtin_amdgcn_raw_buffer_store_b64(v, rs, (unsigned)off, 0, HT_AUX); }
DI f32x2 cswz(f32x2 b) { return (f32x2){-b.y, b.x}; }
DI f32x2 cmul(f32x2 a, f32x2 b, f32x2 bs) {
  const f32x2 t = (f32x2){a.x, a.x} * b;
  return __builtin_elementwise_fma((f32x2){a.y, a.y}, bs, t);
}
DI f32x2 csq(f32x2 a) { return cmul(a, a, cswz(a)); }
DI f32x2 cexp_p(float ar2, float air, float p) {
  const float mag = __builtin_amdgcn_exp2f(ar2 * p);
  const float ph = __builtin_amdgcn_fractf(air * p);
  return (f32x2){mag * __builtin_amdgcn_cosf(ph), mag * __builtin_amdgcn_sinf(ph)};
}
DI unsigned pk16(f32x2 v) { return __builtin_bit_cast(unsigned, __builtin_convertvector(v, f16x2)); }
DI void pow2_scale(float s, float& scale, float& inv) {
  int ex = (s > 0.f) ? __builtin_amdgcn_frexp_expf(s) : 14;
  ex = min(max(ex, -100), 120);
  scale = ldexpf(1.f, 14 - ex);
  inv = ldexpf(1.f, ex - 14);
}
DI int perm23(int r) { return (r & ~12) | ((r & 4) << 1) | ((r & 8) >> 1); }

constexpr int K1_KGEN = 448, K1_LN = 128, K1_WT = 112;
constexpr int K1_GRID = K1_KGEN + K1_LN + K1_WT;
constexpr int KG_RS = 272;
constexpr int KG_WAVE = 32 * KG_RS;
constexpr int KG_PART = 4 * KG_WAVE;
constexpr int K1_LDS = KG_PART + 4 * 48 * 16 * 4 + 64;

struct KTab { unsigned short v[2048]; };
constexpr KTab make_ktab() {
  KTab t{};
  for (int i = 0; i < 2048; ++i) {
    const int l = 2047 - i;
    int e = 0;
    while ((l >> (8 + e)) != 0) ++e;
    int p = l;
    if (e > 0) {
      const int q = l >> e, rem = l & ((1 << e) - 1), half = 1 << (e - 1);
      p = (q + ((rem > half || (rem == half && (q & 1))) ? 1 : 0)) << e;
    }
    int e2 = 0;
    while ((p >> (8 + e2)) != 0) ++e2;
    if (e2 > 3) e2 = 3;
    const int row = 8 * e2 + ((p >> 7) << (3 - e2)) + ((p & 7) >> e2);
    const int col = (p & 127) >> 3;
    t.v[i] = (unsigned short)(row * 16 + col);
  }
  return t;
}
__device__ const KTab KTAB = make_ktab();

DI void k1_kgen(char* smem, int d, const u16* __restrict__ lar_p, const u16* __restrict__ aim_p,
                const u16* __restrict__ cr_p, const u16* __restrict__ ci_p, const u16* __restrict__ ldt_p,
                char* __restrict__ ws) {
  const int tid = threadIdx.x, lane = tid & 63;
  const int kq = __builtin_amdgcn_readfirstlane(tid >> 6);
  STAMP_BASE(ws, 0); STAMP(0);
  const int idx = d * N_ + tid;
  const float lar = bf2f(lar_p[idx]), aim = bf2f(aim_p[idx]), cr = bf2f(cr_p[idx]), ci = bf2f(ci_p[idx]);
  const float ldt = bf2f(ldt_p[d]);
  const u32x4 tab = *(const u32x4*)(&KTAB.v[tid * 8]);
  const float LOG2E = 1.4426950408889634f;
  const float dt = rbf(__builtin_amdgcn_exp2f(ldt * LOG2E));
  const float are = -rbf(__builtin_amdgcn_exp2f(lar * LOG2E));
  const float ar = are * dt, ai = aim * dt;
  const float Cr = cr * dt, Ci = ci * dt;
  float* red = (float*)(smem + K1_LDS - 64);
  const float sw = wave_sum(2.f * (fabsf(Cr) + fabsf(Ci)));
  float wscale, winv;
  pow2_scale(sw, wscale, winv);
  if (lane == 0) red[kq] = sw;
  STAMP(1);
  const float ar2 = ar * LOG2E, air = ai * 0.15915494309189535f;
  char* wb = smem + kq * KG_WAVE + lane * 4;
  char* hb = wb + 16 * KG_RS;
  const int r16 = lane & 15, gq = lane >> 4;
  const char* base = smem + kq * KG_WAVE + r16 * KG_RS + gq * 16;
  f32x4 acc[3] = {};
  f16x8 bfr[4];
#define CM_A(t, a, b) const f32x2 t = (f32x2){(a).x, (a).x} * (b)
#define CM_B(t, a, bs) __builtin_elementwise_fma((f32x2){(a).y, (a).y}, bs, t)
  f32x2 Lo[16], g[8], R[8], z[9];
  const f32x2 E1 = cexp_p(ar2, air, 1.f);
  Lo[1] = cexp_p(ar2, -air, 8.f);
  R[1] = cexp_p(ar2, air, 128.f);
  g[0] = (f32x2){2.f * wscale * Cr, 2.f * wscale * Ci};
  const f32x2 g0s = cswz(g[0]), R1s = cswz(R[1]);
  f32x2 E2, E4, R8, R16;
  {
    const f32x2 E1s = cswz(E1), L1s = cswz(Lo[1]);
    CM_A(t0, E1, E1); CM_A(t1, Lo[1], Lo[1]); CM_A(t2, R[1], R[1]); CM_A(t3, E1, g[0]);
    SCHED_FENCE();
    E2 = CM_B(t0, E1, E1s); Lo[2] = CM_B(t1, Lo[1], L1s); R[2] = CM_B(t2, R[1], R1s); g[1] = CM_B(t3, E1, g0s);
    SCHED_FENCE();
  }
  const f32x2 E2s = cswz(E2), L2s = cswz(Lo[2]), R2s = cswz(R[2]);
  {
    CM_A(t0, E2, E2); CM_A(t1, Lo[2], Lo[2]); CM_A(t2, R[2], R[2]); CM_A(t3, g[0], E2); CM_A(t4, g[1], E2);
    CM_A(t5, Lo[1], Lo[2]); CM_A(t6, R[1], R[2]);
    SCHED_FENCE();
    E4 = CM_B(t0, E2, E2s); Lo[4] = CM_B(t1, Lo[2], L2s); R[4] = CM_B(t2, R[2], R2s); g[2] = CM_B(t3, g[0], E2s);
    g[3] = CM_B(t4, g[1], E2s); Lo[3] = CM_B(t5, Lo[1], L2s); R[3] = CM_B(t6, R[1], R2s);
    SCHED_FENCE();
  }
  const f32x2 E4s = cswz(E4), L4s = cswz(Lo[4]), R4s = cswz(R[4]);
  {
    f32x2 t[12];
#pragma unroll
    for (int k = 0; k < 4; ++k) {
      t[k] = (f32x2){g[k].x, g[k].x} * E4;
      t[4 + k] = (f32x2){Lo[k + 1].x, Lo[k + 1].x} * Lo[4];
      t[8 + k] = (f32x2){R[k + 1].x, R[k + 1].x} * R[4];
    }
    SCHED_FENCE();
#pragma unroll
    for (int k = 0; k < 4; ++k) {
      g[4 + k] = CM_B(t[k], g[k], E4s);
      Lo[5 + k] = CM_B(t[4 + k], Lo[k + 1], L4s);
      if (k < 3) R[5 + k] = CM_B(t[8 + k], R[k + 1], R4s); else R8 = CM_B(t[8 + k], R[4], R4s);
    }
    SCHED_FENCE();
  }
  {
    const f32x2 L8s = cswz(Lo[8]), R8s = cswz(R8), R3s = cswz(R[3]), g4s = cswz(g[4]);
    f32x2 t[33];
#pragma unroll
    for (int k = 1; k < 8; ++k) t[24 + k] = (f32x2){Lo[k].x, Lo[k].x} * Lo[8];
    t[24] = (f32x2){g[0].x, g[0].x} * R8;
    t[32] = (f32x2){R8.x, R8.x} * R8;
#pragma unroll
    for (int c = 0; c < 8; ++c) t[c] = (f32x2){g[c].x, g[c].x} * R[1];
#pragma unroll
    for (int c = 0; c < 4; ++c) { t[8 + c] = (f32x2){g[2 * c].x, g[2 * c].x} * R[2]; t[12 + c] = (f32x2){g[2 * c].x, g[2 * c].x} * R[3]; }
#pragma unroll
    for (int h = 0; h < 4; ++h) { t[16 + 2 * h] = (f32x2){R[4 + h].x, R[4 + h].x} * g[0]; t[17 + 2 * h] = (f32x2){R[4 + h].x, R[4 + h].x} * g[4]; }
    SCHED_FENCE();
    z[0] = CM_B(t[24], g[0], R8s);
    R16 = CM_B(t[32], R8, R8s);
#pragma unroll
    for (int k = 1; k < 8; ++k) Lo[8 + k] = CM_B(t[24 + k], Lo[k], L8s);
#pragma unroll
    for (int c = 0; c < 8; ++c) t[c] = CM_B(t[c], g[c], R1s);
#pragma unroll
    for (int c = 0; c < 4; ++c) { t[8 + c] = CM_B(t[8 + c], g[2 * c], R2s); t[12 + c] = CM_B(t[12 + c], g[2 * c], R3s); }
#pragma unroll
    for (int h = 0; h < 4; ++h) { t[16 + 2 * h] = CM_B(t[16 + 2 * h], R[4 + h], g0s); t[17 + 2 * h] = CM_B(t[17 + 2 * h], R[4 + h], g4s); }
    SCHED_FENCE();
    *(unsigned*)(wb) = 0x00003c00u;
#pragma unroll
    for (int k = 1; k < 16; ++k) *(unsigned*)(wb + k * KG_RS) = pk16(Lo[k]);
#pragma unroll
    for (int c = 0; c < 8; ++c) *(unsigned*)(hb + c * KG_RS) = pk16(g[c]);
#pragma unroll
    for (int i = 0; i < 8; ++i) *(unsigned*)(hb + (8 + i) * KG_RS) = pk16(t[i]);
#pragma unroll
    for (int ks = 0; ks < 4; ++ks) {
      bfr[ks] = *(const f16x8*)(base + ks * 64);
      acc[0] = __builtin_amdgcn_mfma_f32_16x16x32_f16(*(const f16x8*)(base + 16 * KG_RS + ks * 64), bfr[ks], acc[0], 0, 0, 0);
    }
#pragma unroll
    for (int i = 0; i < 16; ++i) *(unsigned*)(hb + i * KG_RS) = pk16(t[8 + i]);
#pragma unroll
    for (int ks = 0; ks < 4; ++ks)
      acc[1] = __builtin_amdgcn_mfma_f32_16x16x32_f16(*(const f16x8*)(base + 16 * KG_RS + ks * 64), bfr[ks], acc[1], 0, 0, 0);
    SCHED_FENCE();
  }
  {
    const f32x2 z0s = cswz(z[0]);
    f32x2 t[9];
#pragma unroll
    for (int k = 1; k < 8; ++k) t[k] = (f32x2){R[k].x, R[k].x} * z[0];
    t[8] = (f32x2){R16.x, R16.x} * g[0];
    SCHED_FENCE();
#pragma unroll
    for (int k = 1; k < 8; ++k) z[k] = CM_B(t[k], R[k], z0s);
    z[8] = CM_B(t[8], R16, g0s);
    SCHED_FENCE();
#pragma unroll
    for (int h = 0; h < 9; ++h) *(unsigned*)(hb + h * KG_RS) = pk16(z[h]);
#pragma unroll
    for (int ks = 0; ks < 4; ++ks)
      acc[2] = __builtin_amdgcn_mfma_f32_16x16x32_f16(*(const f16x8*)(base + 16 * KG_RS + ks * 64), bfr[ks], acc[2], 0, 0, 0);
  }
  STAMP(2);
  {
    float* part = (float*)(smem + KG_PART) + kq;
#pragma unroll
    for (int m = 0; m < 3; ++m)
#pragma unroll
      for (int j = 0; j < 4; ++j) part[((16 * m + 4 * gq + j) * 16 + r16) * 4] = acc[m][j] * winv;
  }
  STAMP(3);
  __syncthreads();
  STAMP(4);
  {
    float scale, inv_scale;
    pow2_scale((red[0] + red[1]) + (red[2] + red[3]), scale, inv_scale);
    const f32x4* part = (const f32x4*)(smem + KG_PART);
    unsigned pk[4];
#pragma unroll
    for (int j = 0; j < 4; ++j) {
      const f32x4 p0 = part[tab[j] & 0xffffu], p1 = part[tab[j] >> 16];
      pk[j] = pack_f16(((p0[0] + p0[1]) + (p0[2] + p0[3])) * scale, ((p1[0] + p1[1]) + (p1[2] + p1[3])) * scale);
    }
    const __amdgpu_buffer_rsrc_t rs = ws_rsrc(ws);
    const size_t kr = OFF_KR + (size_t)d * KR_LEN * 2;
    wst128k(rs, kr + tid * 16, (u32x4){pk[0], pk[1], pk[2], pk[3]});
    if (tid < 32) wst128k(rs, kr + 4096 + tid * 16, (u32x4){0u, 0u, 0u, 0u});
    if (tid == 0) ((float*)(ws + OFF_KS))[d] = inv_scale;
  }
  STAMP(5); STAMPW(14);
}

DI float reduce4(const float (&a)[4]) {
  float v[2];
#pragma unroll
  for (int i = 0; i < 2; ++i) {
    auto r = __builtin_amdgcn_permlane32_swap(__float_as_uint(a[i]), __float_as_uint(a[i + 2]), false, false);
    v[i] = __uint_as_float(r[0]) + __uint_as_float(r[1]);
  }
  auto r = __builtin_amdgcn_permlane16_swap(__float_as_uint(v[0]), __float_as_uint(v[1]), false, false);
  float x = __uint_as_float(r[0]) + __uint_as_float(r[1]);
  x = dpp_add<0xB1>(x);
  x = dpp_add<0x4E>(x);
  x = dpp_add<0x141>(x);
  x = dpp_add<0x140>(x);
  return x;
}
DI float bcast_row(float w, int t) { return __uint_as_float(__builtin_amdgcn_readlane(__float_as_uint(w), 16 * t)); }

constexpr int LN_PL = 448 * 32 + 56 * 16;
DI void k1_ln(char* smem, int blk, const u16* __restrict__ x, char* __restrict__ ws) {
  const int tid = threadIdx.x, lane = tid & 63, wv = tid >> 6;
  STAMP_BASE(ws, 0); STAMP(0);
  const int tok0 = blk * 16 + wv * 4;
  const bool act = lane < 56;
  u32x4 xr[4];
#pragma unroll
  for (int t = 0; t < 4; ++t) {
    xr[t] = (u32x4){0u, 0u, 0u, 0u};
    if (act) xr[t] = *(const u32x4*)(x + (size_t)(tok0 + t) * D_ + lane * 8);
  }
  f32x2 xf[4][4];
  float part[4];
#pragma unroll
  for (int t = 0; t < 4; ++t) {
#pragma unroll
    for (int j = 0; j < 4; ++j) xf[t][j] = (f32x2){bflo(xr[t][j]), bfhi(xr[t][j])};
    const f32x2 s2 = (xf[t][0] + xf[t][1]) + (xf[t][2] + xf[t][3]);
    part[t] = s2.x + s2.y;
  }
  const float wm = reduce4(part) * (1.f / D_);
#pragma unroll
  for (int t = 0; t < 4; ++t) {
    const float m = bcast_row(wm, t);
    f32x2 q2 = {0.f, 0.f};
#pragma unroll
    for (int j = 0; j < 4; ++j) { const f32x2 dd = xf[t][j] - (f32x2){m, m}; q2 = __builtin_elementwise_fma(dd, dd, q2); }
    part[t] = act ? q2.x + q2.y : 0.f;
  }
  const float wq = reduce4(part);
  const float sd = rbf(sqrtf(rbf(rbf(wq * (1.f / D_)) + 1e-5f)));
  const float wr = 1.f / sd;
  const float wb = rbf(wm);
  STAMP(1);
  f32x2 un[4][4];
#pragma unroll
  for (int t = 0; t < 4; ++t) {
    const float mb = bcast_row(wb, t), ri = bcast_row(wr, t);
#pragma unroll
    for (int j = 0; j < 4; ++j) {
      const f32x2 dd = xf[t][j] - (f32x2){mb, mb};
      const unsigned pb = pack_bf16(dd.x, dd.y);
      un[t][j] = (f32x2){bflo(pb), bfhi(pb)} * (f32x2){ri, ri};
    }
  }
  if (act) {
    char* ub = smem + lane * (8 * 32 + 16) + wv * 8;
#pragma unroll
    for (int j = 0; j < 8; ++j) {
      u32x2 pu, px;
#pragma unroll
      for (int t = 0; t < 2; ++t) {
        pu[t] = pack_bf16(un[2 * t][j >> 1][j & 1], un[2 * t + 1][j >> 1][j & 1]);
        const unsigned a = xr[2 * t][j >> 1], b = xr[2 * t + 1][j >> 1];
        px[t] = (j & 1) ? __builtin_amdgcn_perm(b, a, 0x07060302u) : __builtin_amdgcn_perm(b, a, 0x05040100u);
      }
      *(u32x2*)(ub + j * 32) = pu;
      *(u32x2*)(ub + LN_PL + j * 32) = px;
    }
  }
  STAMP(2);
  __syncthreads();
  STAMP(3);
  const __amdgpu_buffer_rsrc_t wrs = ws_rsrc(ws);
#pragma unroll
  for (int i = 0; i < 4; ++i) {
    const int ch = i * 128 + (tid >> 1), v = tid & 1;
    if (ch < D_) {
      const int o = ch * 32 + (ch >> 3) * 16 + v * 16;
      const u32x4 a = *(const u32x4*)(smem + o);
      const u32x4 b = *(const u32x4*)(smem + LN_PL + o);
      __builtin_amdgcn_raw_buffer_store_b128(a, wrs, (unsigned)(OFF_UT + ((size_t)ch * L_ + blk * 16 + v * 8) * 2), 0, LN_AUX);
      __builtin_amdgcn_raw_buffer_store_b128(b, wrs, (unsigned)(OFF_XT + ((size_t)ch * L_ + blk * 16 + v * 8) * 2), 0, LN_AUX);
    }
  }
  STAMP(4); STAMPW(14);
}

typedef __attribute__((address_space(3))) bf16x4* lds_bf16x4_p;
DI void k1_wtile(char* smem, const u16* __restrict__ src, int ldn, size_t dsto, const u16* __restrict__ b1s, char* ws) {
  const int tid = threadIdx.x, lane = tid & 63, wv = tid >> 6;
  const __amdgpu_buffer_rsrc_t rs = ws_rsrc(ws);
  STAMP_BASE(ws, 0); STAMP(0);
  u32x4 v[7];
#pragma unroll
  for (int i = 0; i < 7; ++i) {
    const int c = i * 256 + tid, k = c >> 2, q = c & 3;
    v[i] = *(const u32x4*)(src + (size_t)k * ldn + q * 8);
  }
  unsigned b1v = 0;
  if (b1s) b1v = b1s[perm23(tid & 31)];
  SCHED_FENCE();
  {
    const int q = tid & 3;
    char* dst = smem + (tid >> 2) * 64 + 32 * (q >> 1) + 8 * (q & 1);
#pragma unroll
    for (int i = 0; i < 7; ++i) {
      *(u32x2*)(dst + i * 4096) = (u32x2){v[i][0], v[i][1]};
      *(u32x2*)(dst + i * 4096 + 16) = (u32x2){v[i][2], v[i][3]};
    }
  }
  __syncthreads();
  STAMP(1);
  {
    const int hh = lane >> 5, blk = (lane >> 4) & 1, q = (lane & 15) >> 2, p = lane & 3;
    const char* rd = smem + (8 * hh + q) * 64 + 32 * blk + 8 * p + wv * 1024;
#pragma unroll
    for (int i = 0; i < 7; ++i) {
      const bf16x4 lo = __builtin_amdgcn_ds_read_tr16_b64_v4bf16((lds_bf16x4_p)(rd + i * 4096));
      const bf16x4 hi = __builtin_amdgcn_ds_read_tr16_b64_v4bf16((lds_bf16x4_p)(rd + i * 4096 + 256));
      const bf16x8 f = __builtin_shufflevector(lo, hi, 0, 1, 2, 3, 4, 5, 6, 7);
      wst128(rs, dsto + (size_t)((i * 4 + wv) * 64 + lane) * 16, __builtin_bit_cast(u32x4, f));
    }
  }
  STAMP(2);
  if (b1s) {
    const int cp = tid & 15, kg = tid >> 4;
    f32x2 sacc = {0.f, 0.f};
#pragma unroll
    for (int k = 0; k < 28; ++k) {
      const unsigned w = *(const unsigned*)(smem + (kg * 28 + k) * 64 + cp * 4);
      sacc += (f32x2){bflo(w), bfhi(w)};
    }
    f32x2* red = (f32x2*)(smem + 448 * 64);
    red[tid] = sacc;
    __syncthreads();
    if (tid < 64) {
      const float* redf = (const float*)red;
      float a = 0.f;
#pragma unroll
      for (int g = 0; g < 16; ++g) a += redf[g * 32 + (tid & 31)];
      const float hi = rbf(a);
      const unsigned w0 = pack_bf16(hi, a - hi);
      wst128(rs, dsto + (size_t)(28 * 64 + tid) * 16, (u32x4){tid < 32 ? w0 : 0u, tid < 32 ? b1v : 0u, 0u, 0u});
    }
  }
  STAMP(3); STAMPW(14);
}

__global__ __launch_bounds__(256, 3) void k1_kernel(const u16* __restrict__ x, const u16* __restrict__ lar,
                                                    const u16* __restrict__ aim, const u16* __restrict__ cr,
                                                    const u16* __restrict__ ci, const u16* __restrict__ ldt,
                                                    const u16* __restrict__ w1, const u16* __restrict__ b1,
                                                    const u16* __restrict__ w2, char* __restrict__ ws) {
  __shared__ __attribute__((aligned(16))) char smem[K1_LDS];
  int b = blockIdx.x;
  if (b < K1_LN) {
    if (b == 0 && threadIdx.x < 65) ((unsigned*)(ws + OFF_CNT))[threadIdx.x * 64] = 0u;
    k1_ln(smem, b, x, ws);
    return;
  }
  b -= K1_LN;
  if (b < K1_KGEN) { k1_kgen(smem, b, lar, aim, cr, ci, ldt, ws); return; }
  b -= K1_KGEN;
  if (b < 56) {
    k1_wtile(smem, w1 + b * 32, F_, OFF_W1 + (size_t)b * W1_FRAGS * 1024, b1 + b * 32, ws);
  } else {
    const int dt = (b - 56) >> 2, kc = (b - 56) & 3;
    k1_wtile(smem, w2 + (size_t)kc * 448 * D_ + dt * 32, D_, OFF_W2 + ((size_t)dt * 112 + kc * 28) * 1024, nullptr, ws);
  }
}

constexpr int CV_CS = 4160;
constexpr int CV_KOFF = 8 * CV_CS;
constexpr int CV_CH = CV_KOFF + KR_LEN * 2;
constexpr int K3_TILE = 448 * 128;
constexpr int K3_RED = 8 * 7 * 2048;
constexpr int K3_STAT = K3_RED;
constexpr int K3_LDS = K3_STAT + 4 * 64 * 8;
static_assert(2 * CV_CH <= K3_LDS && 8 * 3 * 1024 <= CV_CH, "lds");
typedef __attribute__((address_space(1))) unsigned gu32;

DI bf16x8 k3_bfrag(const char* smem, unsigned lane_off0, unsigned lane_off1, int off) {
  const bf16x4 lo = __builtin_amdgcn_ds_read_tr16_b64_v4bf16((lds_bf16x4_p)(smem + lane_off0 + off));
  const bf16x4 hi = __builtin_amdgcn_ds_read_tr16_b64_v4bf16((lds_bf16x4_p)(smem + lane_off1 + off));
  return __builtin_shufflevector(lo, hi, 0, 1, 2, 3, 4, 5, 6, 7);
}
#define W1_EARLY 5
#define H1_AUX 16
#define POLL_SLEEP 1
#define H1_WIN 6
template <int N> DI void wait_vm() { asm volatile("s_waitcnt vmcnt(%0)" ::"i"(N) : "memory"); }

__global__ __launch_bounds__(512, 2) void k2_kernel(const u16* __restrict__ dskip, const u16* __restrict__ b2,
                                                    char* __restrict__ ws, u16* __restrict__ out) {
  __shared__ __attribute__((aligned(16))) char smem[K3_LDS];
  const int tid = threadIdx.x, lane = tid & 63;
  const int wv = __builtin_amdgcn_readfirstlane(tid >> 6);
  const int xcd = blockIdx.x & 7, idx = blockIdx.x >> 3;
  const int grp = 4 * xcd + idx / 7, j = idx % 7;
  const int tok0 = grp * 64, ft = j * 8 + wv;
  const int sh = (wv >> 1) & 1;
  const int s2 = wv >> 2;
  STAMP_BASE(ws, 2); STAMP(0);
  const __amdgpu_buffer_rsrc_t wrs = ws_rsrc(ws);
  const bf16x8* w1f = (const bf16x8*)(ws + OFF_W1) + (size_t)ft * W1_FRAGS * 64 + lane;
  const bf16x8* w1a = w1f + sh * 128;
  const bf16x8* w1b = w1f + (1 - sh) * 128;
  bf16x8 A[28];
#define ISSUEA(c) do { A[4 * (c)] = w1a[(4 * (c)) * 64]; A[4 * (c) + 1] = w1a[(4 * (c) + 1) * 64]; \
    A[4 * (c) + 2] = w1b[(4 * (c)) * 64]; A[4 * (c) + 3] = w1b[(4 * (c) + 1) * 64]; } while (0)
#define LDA(n) ((((n) & 2) == 0) ? w1a[(4 * ((n) >> 2) + ((n) & 1)) * 64] : w1b[(4 * ((n) >> 2) + ((n) & 1)) * 64])
  bf16x8 Aext;
  {
    const int d = 2 * blockIdx.x + (wv >> 2), cw = wv & 3, m0 = tid & 255;
    char* cs = smem + (wv >> 2) * CV_CH;
    const u16* uT = (const u16*)(ws + OFF_UT) + (size_t)d * L_;
    const u16* xT = (const u16*)(ws + OFF_XT) + (size_t)d * L_;
    const char* kr = ws + OFF_KR + (size_t)d * KR_LEN * 2;
    u32x4 c0 = {0u, 0u, 0u, 0u};
    if (m0 > 0) c0 = *(const u32x4*)(uT + (m0 - 1) * 8);
    const u32x4 c1 = *(const u32x4*)(uT + m0 * 8);
    const u32x4 k0 = *(const u32x4*)(kr + m0 * 16);
    u32x4 k1 = {0u, 0u, 0u, 0u};
    if (m0 < 32) k1 = *(const u32x4*)(kr + 4096 + m0 * 16);
    const int c16 = lane & 15, g4 = lane >> 4;
    u32x2 ue[2], xe[2];
#pragma unroll
    for (int i = 0; i < 2; ++i) {
      const int pp = 256 * (2 * cw + i) + 16 * c16 + 4 * g4;
      ue[i] = *(const u32x2*)(uT + pp);
      xe[i] = *(const u32x2*)(xT + pp);
    }
    const float inv_scale = ((const float*)(ws + OFF_KS))[d];
    const float dsk = bf2f(dskip[d]);
    SCHED_FENCE();
    *(u32x4*)(cs + CV_KOFF + m0 * 16) = k0;
    if (m0 < 32) *(u32x4*)(cs + CV_KOFF + 4096 + m0 * 16) = k1;
    if (m0 < 8) *(u32x4*)(cs + m0 * CV_CS) = (u32x4){0u, 0u, 0u, 0u};
    {
      unsigned pq[8];
#pragma unroll
      for (int e = 0; e < 4; ++e) {
        pq[e] = pack_f16(bflo(c0[e]), bfhi(c0[e]));
        pq[4 + e] = pack_f16(bflo(c1[e]), bfhi(c1[e]));
      }
      unsigned qq[7];
#pragma unroll
      for (int i = 0; i < 7; ++i) qq[i] = __builtin_amdgcn_alignbit(pq[i + 1], pq[i], 16);
      char* dst = cs + (m0 + 1) * 16;
#pragma unroll
      for (int rho = 0; rho < 8; ++rho) {
        const int k = (rho + 1) >> 1;
        const u32x4 w = (rho & 1) ? (u32x4){pq[k], pq[k + 1], pq[k + 2], pq[k + 3]} : (u32x4){qq[k], qq[k + 1], qq[k + 2], qq[k + 3]};
        *(u32x4*)(dst + rho * CV_CS) = w;
      }
    }
    __syncthreads();
    STAMP(9);
    f32x4 cacc[8] = {};
    const char* abase = cs + (c16 & 7) * CV_CS + ((c16 & 8) + 8 * g4) * 2;
    const char* bbase = cs + CV_KOFF + (2032 - 16 * c16 + 8 * g4) * 2;
#pragma unroll
    for (int si = 0; si < 2; ++si) {
      const int sg = 2 * cw + si;
      f16x8 CA[8], CB[8];
#pragma unroll
      for (int i = 0; i < 8; ++i) {
        CA[i] = *(const f16x8*)(abase + sg * 64 + i * 512);
        CB[i] = *(const f16x8*)(bbase + sg * 64 - i * 512);
      }
#pragma unroll
      for (int t = 0; t < 8; ++t) {
#pragma unroll
        for (int ia = 0; ia <= t; ++ia)
          cacc[t] = __builtin_amdgcn_mfma_f32_16x16x32_f16(CA[ia], CB[t - ia], cacc[t], 0, 0, 0);
        if (t >= 3) {
          const int n0 = (si * 5 + (t - 3)) * 2;
          if (n0 < 4 * W1_EARLY) { A[n0] = LDA(n0); A[n0 + 1] = LDA(n0 + 1); }
          SCHED_FENCE();
        }
      }
      SCHED_FENCE();
    }
    __syncthreads();
    f32x4* red = (f32x4*)cs;
    f32x4 own[2] = {};
#pragma unroll
    for (int t = 0; t < 8; ++t) {
      if ((t >> 1) == cw) own[t & 1] = cacc[t];
      else red[(t * 3 + (cw > (t >> 1) ? cw - 1 : cw)) * 64 + lane] = cacc[t];
    }
    __syncthreads();
#pragma unroll
    for (int i = 0; i < 2; ++i) {
      const int t = 2 * cw + i;
      const f32x4 sum = (own[i] + red[(t * 3) * 64 + lane]) + (red[(t * 3 + 1) * 64 + lane] + red[(t * 3 + 2) * 64 + lane]);
      float h[4];
#pragma unroll
      for (int e = 0; e < 4; ++e) {
        const float uu = (e & 1) ? bfhi(ue[i][e >> 1]) : bflo(ue[i][e >> 1]);
        const float xx = (e & 1) ? bfhi(xe[i][e >> 1]) : bflo(xe[i][e >> 1]);
        const float y = rbf(sum[e] * inv_scale);
        const float tt = rbf(y + rbf(uu * dsk));
        h[e] = xx + tt;
      }
      __builtin_amdgcn_raw_buffer_store_b64((u32x2){pack_bf16(h[0], h[1]), pack_bf16(h[2], h[3])}, wrs,
                                            (unsigned)(OFF_HT + ((size_t)d * HT_LD + 256 * t + 16 * c16 + 4 * g4) * 2), 0, 16);
    }
  }
  STAMP(10);
  wait_vm<0>();
  __builtin_amdgcn_s_barrier();
  asm volatile("" ::: "memory");
  STAMP(11);
  if (wv == 0) {
    gu32* gc = (gu32*)(ws + OFF_CNT) + 32 * 64;
    unsigned last = 0;
    if (lane == 0) {
      if (__hip_atomic_fetch_add(gc + (blockIdx.x & 15) * 64, 1u, __ATOMIC_RELAXED, __HIP_MEMORY_SCOPE_AGENT) == 13u)
        last = __hip_atomic_fetch_add(gc + 16 * 64, 1u, __ATOMIC_RELAXED, __HIP_MEMORY_SCOPE_AGENT) == 15u;
    }
    if (__builtin_amdgcn_readfirstlane(last)) {
      if (lane < 16) __hip_atomic_store(gc + (17 + lane) * 64, 1u, __ATOMIC_RELAXED, __HIP_MEMORY_SCOPE_AGENT);
    } else if (lane == 0) {
      for (unsigned spins = 0; spins < (1u << 20); ++spins) {
        if (__hip_atomic_load(gc + (17 + (blockIdx.x & 15)) * 64, __ATOMIC_RELAXED, __HIP_MEMORY_SCOPE_AGENT) != 0u) break;
        __builtin_amdgcn_s_sleep(POLL_SLEEP);
      }
    }
    __builtin_amdgcn_fence(__ATOMIC_ACQUIRE, "wavefront");
  }
  SCHED_FENCE();
#pragma unroll
  for (int c = W1_EARLY; c < 7; ++c) ISSUEA(c);
  Aext = w1f[28 * 64];
  SCHED_FENCE();
  __builtin_amdgcn_s_barrier();
  __builtin_amdgcn_fence(__ATOMIC_ACQUIRE, "wavefront");
  asm volatile("" ::: "memory");
  STAMP(12);
  const unsigned hsrc = (unsigned)(OFF_HT + ((size_t)(tid >> 3) * HT_LD + tok0 + (tid & 7) * 8) * 2);
  const __amdgpu_buffer_rsrc_t w2rs = __builtin_amdgcn_make_buffer_rsrc((void*)(ws + OFF_W2), 0, D_ * F_ * 2, 0x00020000);
  const unsigned w2v = ((2 * j) * 112 * 64 + lane) * 16;
#define KSLOT(sl) ((sl) < 2 ? 16 * j + 2 * wv + (sl) : (12 * wv + (sl) - 2) + ((12 * wv + (sl) - 2) >= 16 * j ? 16 : 0))
#define LDW2(sl, dt) __builtin_bit_cast(bf16x8, __builtin_amdgcn_raw_buffer_load_b128(w2rs, w2v + (dt) * 112 * 1024, KSLOT(sl) * 1024, 0))
  u32x4 st[7];
  bf16x8 a0[14], a1[14];
#define ISSUE2(c) do { a0[2 * (c)] = LDW2(2 * (c), 0); a1[2 * (c)] = LDW2(2 * (c), 1); \
    a0[2 * (c) + 1] = LDW2(2 * (c) + 1, 0); a1[2 * (c) + 1] = LDW2(2 * (c) + 1, 1); } while (0)
#pragma unroll
  for (int c = 0; c < 7; ++c) st[c] = __builtin_amdgcn_raw_buffer_load_b128(wrs, hsrc, 64 * c * HT_LD * 2, 16);
  SCHED_FENCE();
  const int hh = lane >> 5, col = lane & 31;
  const unsigned q = (lane & 15) >> 2, p = lane & 3, blk = (lane >> 4) & 1;
  char* lw = smem + 128 * (tid >> 3) + 16 * ((tid & 7) ^ (((tid >> 4) & 1) << 2));
  unsigned oa[2][2], ob[2][2];
  {
    const unsigned r0 = 8 * hh + q, r1 = 8 * hh + 4 + q, sw = ((q >> 1) & 1) << 2;
#pragma unroll
    for (int it = 0; it < 2; ++it) {
      const unsigned c = (wv & 1) ^ it;
      const unsigned chk = (4 * c + 2 * blk + (p >> 1)) ^ sw;
      const unsigned x0 = 128 * r0 + 16 * chk + 8 * (p & 1), x1 = 128 * r1 + 16 * chk + 8 * (p & 1);
      oa[it][0] = x0 + sh * 4096; oa[it][1] = x1 + sh * 4096;
      ob[it][0] = x0 + (1 - sh) * 4096; ob[it][1] = x1 + (1 - sh) * 4096;
    }
  }
  f32x16 acc[2] = {};
  float ssum = 0.f, ssq = 0.f;
  const bf16x2 ones2 = {(__bf16)1.0f, (__bf16)1.0f};
#define WRITE1(c) do { *(u32x4*)(lw + (c) * 8192) = st[c]; } while (0)
#define RDB(n, it) ((((n) & 2) == 0) ? k3_bfrag(smem, oa[it][0], oa[it][1], ((n) >> 2) * 8192 + ((n) & 1) * 2048) \
                                      : k3_bfrag(smem, ob[it][0], ob[it][1], ((n) >> 2) * 8192 + ((n) & 1) * 2048))
  bf16x8 Bf[3][2];
  WRITE1(0);
  WRITE1(1);
  __syncthreads();
  STAMP(1);
  Bf[0][0] = RDB(0, 0); Bf[0][1] = RDB(0, 1); Bf[1][0] = RDB(1, 0); Bf[1][1] = RDB(1, 1);
#pragma unroll
  for (int n = 0; n < 28; ++n) {
    const int c = n >> 2, i = n & 3;
    acc[0] = __builtin_amdgcn_mfma_f32_32x32x16_bf16(A[n], Bf[n % 3][0], acc[0], 0, 0, 0);
    if (n + 2 < 28) Bf[(n + 2) % 3][0] = RDB(n + 2, 0);
    if (i < 2) {
      const u32x4 B = __builtin_bit_cast(u32x4, Bf[n % 3][0]);
      const bf16x2 pr = __builtin_bit_cast(bf16x2, s2 ? B[2] : B[0]);
      ssum = __builtin_amdgcn_fdot2_f32_bf16(pr, ones2, ssum, false);
      ssq = __builtin_amdgcn_fdot2_f32_bf16(pr, pr, ssq, false);
    }
    SCHED_FENCE();
    acc[1] = __builtin_amdgcn_mfma_f32_32x32x16_bf16(A[n], Bf[n % 3][1], acc[1], 0, 0, 0);
    if (n + 2 < 28) Bf[(n + 2) % 3][1] = RDB(n + 2, 1);
    if (i < 2) {
      const u32x4 B = __builtin_bit_cast(u32x4, Bf[n % 3][0]);
      const bf16x2 pr = __builtin_bit_cast(bf16x2, s2 ? B[3] : B[1]);
      ssum = __builtin_amdgcn_fdot2_f32_bf16(pr, ones2, ssum, false);
      ssq = __builtin_amdgcn_fdot2_f32_bf16(pr, pr, ssq, false);
    }
    if (i == 0 && c + 2 < 7) WRITE1(c + 2);
    if (i == 3) ISSUE2(c);
    SCHED_FENCE();
    if (i == 3 && c + 2 < 7) __syncthreads();
  }
  STAMP(2);
  float2* stats = (float2*)(smem + K3_STAT);
  {
    auto r1 = __builtin_amdgcn_permlane32_swap(__float_as_uint(ssum), __float_as_uint(ssum), false, false);
    ssum = __uint_as_float(r1[0]) + __uint_as_float(r1[1]);
    auto r2 = __builtin_amdgcn_permlane32_swap(__float_as_uint(ssq), __float_as_uint(ssq), false, false);
    ssq = __uint_as_float(r2[0]) + __uint_as_float(r2[1]);
    if (hh == 0) stats[(wv >> 1) * 64 + (wv & 1) * 32 + col] = make_float2(ssum, ssq);
  }
  const int t2 = wv >> 1, hf2 = wv & 1;
  const int c2 = t2 & 1;
  bf16x8 res;
  {
    const unsigned r0 = 8 * hh + q, r1 = 8 * hh + 4 + q, sw = ((q >> 1) & 1) << 2;
    const unsigned chk = (4 * c2 + 2 * blk + (p >> 1)) ^ sw;
    res = k3_bfrag(smem, 128 * r0 + 16 * chk + 8 * (p & 1), 128 * r1 + 16 * chk + 8 * (p & 1), (j * 4 + (t2 >> 1) * 2 + hf2) * 2048);
  }
  __syncthreads();
  float sdv[2], c1v[2], c2v[2];
#pragma unroll
  for (int it = 0; it < 2; ++it) {
    const unsigned c = (wv & 1) ^ it;
    const float2 s0 = stats[c * 32 + col], s1 = stats[64 + c * 32 + col], s2_ = stats[128 + c * 32 + col], s3 = stats[192 + c * 32 + col];
    const float mean = ((s0.x + s1.x) + (s2_.x + s3.x)) * (1.f / D_);
    const float var = fmaxf(__builtin_fmaf(-mean, mean, ((s0.y + s1.y) + (s2_.y + s3.y)) * (1.f / D_)), 0.f);
    const float sd = rbf(sqrtf(rbf(rbf(var) + 1e-5f)));
    const float rs = __builtin_amdgcn_rcpf(sd);
    sdv[it] = sd;
    c1v[it] = rs * -2.3022082f;
    c2v[it] = rs * rs * rs * -0.10294324f;
    const unsigned w0 = pack_bf16(-mean, -mean), w1 = pack_bf16(sd, 0.f);
    const u32x4 bw = {hh ? 0u : w0, hh ? 0u : w1, 0u, 0u};
    acc[it] = __builtin_amdgcn_mfma_f32_32x32x16_bf16(Aext, __builtin_bit_cast(bf16x8, bw), acc[it], 0, 0, 0);
  }
  const __amdgpu_buffer_rsrc_t h1rs = __builtin_amdgcn_make_buffer_rsrc((void*)(ws + OFF_H1), 0, L_ * F_ * 2, 0x00020000);
  u32x4 own[2][2];
#pragma unroll
  for (int it = 0; it < 2; ++it) {
    const unsigned c = (wv & 1) ^ it;
    const unsigned fb = (((grp * 2 + c) * 112 + 2 * ft) * 64 + lane) * 16;
    const f32x2 c1p = {c1v[it], c1v[it]}, c2p = {c2v[it], c2v[it]}, sdp = {sdv[it], sdv[it]};
#pragma unroll
    for (int hf = 0; hf < 2; ++hf) {
      f32x2 a[4], z[4];
#pragma unroll
      for (int e = 0; e < 4; ++e) a[e] = (f32x2){acc[it][8 * hf + 2 * e], acc[it][8 * hf + 2 * e + 1]};
#pragma unroll
      for (int e = 0; e < 4; ++e) z[e] = a[e] * a[e];
#pragma unroll
      for (int e = 0; e < 4; ++e) z[e] = __builtin_elementwise_fma(z[e], c2p, c1p);
#pragma unroll
      for (int e = 0; e < 4; ++e) z[e] = a[e] * z[e];
#pragma unroll
      for (int e = 0; e < 4; ++e) z[e] = (f32x2){__builtin_amdgcn_exp2f(z[e].x), __builtin_amdgcn_exp2f(z[e].y)};
#pragma unroll
      for (int e = 0; e < 4; ++e) z[e] = __builtin_elementwise_fma(z[e], sdp, sdp);
#pragma unroll
      for (int e = 0; e < 4; ++e) z[e] = (f32x2){__builtin_amdgcn_rcpf(z[e].x), __builtin_amdgcn_rcpf(z[e].y)};
#pragma unroll
      for (int e = 0; e < 4; ++e) z[e] = a[e] * z[e];
      const u32x4 pk = {pack_bf16(z[0].x, z[0].y), pack_bf16(z[1].x, z[1].y), pack_bf16(z[2].x, z[2].y), pack_bf16(z[3].x, z[3].y)};
      __builtin_amdgcn_raw_buffer_store_b128(pk, h1rs, fb + hf * 1024, 0, 16);
      own[it][hf] = pk;
      SCHED_FENCE();
    }
  }
  f32x16 acc2[4] = {};
#pragma unroll
  for (int ks = 0; ks < 2; ++ks) {
    const bf16x8 y0 = __builtin_bit_cast(bf16x8, own[0][ks]), y1 = __builtin_bit_cast(bf16x8, own[1][ks]);
    acc2[0] = __builtin_amdgcn_mfma_f32_32x32x16_bf16(a0[ks], y0, acc2[0], 0, 0, 0);
    acc2[1] = __builtin_amdgcn_mfma_f32_32x32x16_bf16(a0[ks], y1, acc2[1], 0, 0, 0);
    acc2[2] = __builtin_amdgcn_mfma_f32_32x32x16_bf16(a1[ks], y0, acc2[2], 0, 0, 0);
    acc2[3] = __builtin_amdgcn_mfma_f32_32x32x16_bf16(a1[ks], y1, acc2[3], 0, 0, 0);
  }
  STAMP(3);
  wait_vm<0>();
  __builtin_amdgcn_s_barrier();
  asm volatile("" ::: "memory");
  STAMP(4);
  gu32* cnt = (gu32*)(ws + OFF_CNT) + grp * 64;
  if (wv == 0) {
    if (lane == 0) {
      __hip_atomic_fetch_add(cnt, 1u, __ATOMIC_RELAXED, __HIP_MEMORY_SCOPE_AGENT);
      for (unsigned spins = 0; spins < (1u << 20); ++spins) {
        if (__hip_atomic_load(cnt, __ATOMIC_RELAXED, __HIP_MEMORY_SCOPE_AGENT) >= 7u) break;
        __builtin_amdgcn_s_sleep(1);
      }
    }
    __builtin_amdgcn_fence(__ATOMIC_ACQUIRE, "wavefront");
  }
  __builtin_amdgcn_s_barrier();
  __builtin_amdgcn_fence(__ATOMIC_ACQUIRE, "wavefront");
  asm volatile("" ::: "memory");
  STAMP(5);
  const unsigned hb0 = (((grp * 2 + (wv & 1)) * 112) * 64 + lane) * 16, hb1 = (((grp * 2 + 1 - (wv & 1)) * 112) * 64 + lane) * 16;
  u32x4 b0[H1_WIN], b1v[H1_WIN];
#pragma unroll
  for (int i = 0; i < H1_WIN; ++i) {
    b0[i] = __builtin_amdgcn_raw_buffer_load_b128(h1rs, hb0, KSLOT(2 + i) * 1024, H1_AUX);
    b1v[i] = __builtin_amdgcn_raw_buffer_load_b128(h1rs, hb1, KSLOT(2 + i) * 1024, H1_AUX);
  }
  const int d0 = j * 64 + (t2 >> 1) * 32 + 16 * hf2 + 8 * hh;
  const u32x4 bz = *(const u32x4*)(b2 + d0);
  SCHED_FENCE();
#pragma unroll
  for (int ks = 0; ks < 12; ++ks) {
    const int sl = ks % H1_WIN;
    const bf16x8 y0 = __builtin_bit_cast(bf16x8, b0[sl]), y1 = __builtin_bit_cast(bf16x8, b1v[sl]);
    acc2[0] = __builtin_amdgcn_mfma_f32_32x32x16_bf16(a0[2 + ks], y0, acc2[0], 0, 0, 0);
    acc2[1] = __builtin_amdgcn_mfma_f32_32x32x16_bf16(a0[2 + ks], y1, acc2[1], 0, 0, 0);
    acc2[2] = __builtin_amdgcn_mfma_f32_32x32x16_bf16(a1[2 + ks], y0, acc2[2], 0, 0, 0);
    acc2[3] = __builtin_amdgcn_mfma_f32_32x32x16_bf16(a1[2 + ks], y1, acc2[3], 0, 0, 0);
    if (ks + H1_WIN < 12) {
      b0[sl] = __builtin_amdgcn_raw_buffer_load_b128(h1rs, hb0, KSLOT(2 + ks + H1_WIN) * 1024, H1_AUX);
      b1v[sl] = __builtin_amdgcn_raw_buffer_load_b128(h1rs, hb1, KSLOT(2 + ks + H1_WIN) * 1024, H1_AUX);
    }
    SCHED_FENCE();
  }
  STAMP(6);
  f32x4* red = (f32x4*)smem;
  f32x4 own0 = {}, own1 = {};
#pragma unroll
  for (int a = 0; a < 8; ++a) {
    const int u = (a ^ (2 * (wv & 1)));
    const f32x4 v0 = {acc2[a >> 1][8 * (a & 1)], acc2[a >> 1][8 * (a & 1) + 1], acc2[a >> 1][8 * (a & 1) + 2], acc2[a >> 1][8 * (a & 1) + 3]};
    const f32x4 v1 = {acc2[a >> 1][8 * (a & 1) + 4], acc2[a >> 1][8 * (a & 1) + 5], acc2[a >> 1][8 * (a & 1) + 6], acc2[a >> 1][8 * (a & 1) + 7]};
    if (u == wv) { own0 = v0; own1 = v1; }
    else {
      const int slot = u * 7 + (wv > u ? wv - 1 : wv);
      red[(slot * 2) * 64 + lane] = v0;
      red[(slot * 2 + 1) * 64 + lane] = v1;
    }
  }
  __syncthreads();
  STAMP(7);
  {
    f32x4 s0[7], s1[7];
#pragma unroll
    for (int i = 0; i < 7; ++i) { s0[i] = red[((wv * 7 + i) * 2) * 64 + lane]; s1[i] = red[((wv * 7 + i) * 2 + 1) * 64 + lane]; }
    const f32x4 o0 = ((own0 + s0[0]) + (s0[1] + s0[2])) + ((s0[3] + s0[4]) + (s0[5] + s0[6]));
    const f32x4 o1 = ((own1 + s1[0]) + (s1[1] + s1[2])) + ((s1[3] + s1[4]) + (s1[5] + s1[6]));
    const u32x4 hr = __builtin_bit_cast(u32x4, res);
    unsigned pk[4];
#pragma unroll
    for (int e = 0; e < 4; ++e) {
      const float x0 = (e < 2) ? o0[2 * e] : o1[2 * e - 4], x1 = (e < 2) ? o0[2 * e + 1] : o1[2 * e - 3];
      const unsigned ob_ = pack_bf16(x0, x1);
      const unsigned o2 = pack_bf16(bflo(ob_) + bflo(bz[e]), bfhi(ob_) + bfhi(bz[e]));
      pk[e] = pack_bf16(bflo(hr[e]) + bflo(o2), bfhi(hr[e]) + bfhi(o2));
    }
    const __amdgpu_buffer_rsrc_t ors = __builtin_amdgcn_make_buffer_rsrc((void*)out, 0, L_ * D_ * 2, 0x00020000);
    __builtin_amdgcn_raw_buffer_store_b128((u32x4){pk[0], pk[1], pk[2], pk[3]}, ors, ((tok0 + c2 * 32 + col) * D_ + d0) * 2, 0, OUT_AUX);
  }
  STAMP(8); STAMPW(14);
}

extern "C" void kernel_launch(void* const* d_in, const int* in_sizes, int n_in, void* d_out, int out_size, void* d_ws,
                              size_t ws_size, hipStream_t stream) {
  const u16* x = (const u16*)d_in[0];
  const u16* lar = (const u16*)d_in[1];
  const u16* aim = (const u16*)d_in[2];
  const u16* cr = (const u16*)d_in[3];
  const u16* ci = (const u16*)d_in[4];
  const u16* ldt = (const u16*)d_in[5];
  const u16* dsk = (const u16*)d_in[6];
  const u16* w1 = (const u16*)d_in[8];
  const u16* b1 = (const u16*)d_in[9];
  const u16* w2 = (const u16*)d_in[10];
  const u16* b2 = (const u16*)d_in[11];
  char* ws = (char*)d_ws;
  k1_kernel<<<K1_GRID, 256, 0, stream>>>(x, lar, aim, cr, ci, ldt, w1, b1, w2, ws);
  k2_kernel<<<224, 512, 0, stream>>>(dsk, b2, ws, (u16*)d_out);
}
```

```cpp
#include <hip/hip_runtime.h>
#include <hip/hip_bf16.h>
#include <stdint.h>

typedef __attribute__((ext_vector_type(8))) __bf16 bf16x8;
typedef __attribute__((ext_vector_type(4))) __bf16 bf16x4;
typedef __attribute__((ext_vector_type(2))) __bf16 bf16x2;
typedef __attribute__((ext_vector_type(8))) _Float16 f16x8;
typedef __attribute__((ext_vector_type(2))) _Float16 f16x2;
typedef __attribute__((ext_vector_type(2))) float f32x2;
typedef __attribute__((ext_vector_type(4))) float f32x4;
typedef __attribute__((ext_vector_type(16))) float f32x16;
typedef __attribute__((ext_vector_type(4))) unsigned u32x4;
typedef __attribute__((ext_vector_type(2))) unsigned u32x2;
typedef unsigned short u16;

#define DI __device__ __forceinline__
#define SCHED_FENCE() __builtin_amdgcn_sched_barrier(0)
#define STAMP_BASE(ws, kid)
#define STAMP(i)
#define STAMPW(i)

constexpr int L_ = 2048, D_ = 448, N_ = 256, F_ = 1792;
constexpr int KR_LEN = 2304;

constexpr size_t OFF_UT = 0;
constexpr size_t OFF_XT = OFF_UT + (size_t)D_ * L_ * 2;
constexpr size_t OFF_KR = OFF_XT + (size_t)D_ * L_ * 2;
constexpr size_t OFF_KS = OFF_KR + (size_t)D_ * KR_LEN * 2;
constexpr int W1_FRAGS = 29;
constexpr size_t OFF_CNT = OFF_KS + 2048;
constexpr size_t OFF_W1 = OFF_CNT + 32768;
constexpr size_t OFF_W2 = OFF_W1 + (size_t)56 * W1_FRAGS * 1024;
constexpr int HT_LD = L_ + 128;
constexpr size_t OFF_HT = OFF_W2 + (size_t)D_ * F_ * 2;
constexpr size_t OFF_H1 = OFF_HT + (size_t)D_ * HT_LD * 2;
constexpr size_t WS_END = OFF_H1 + (size_t)L_ * F_ * 2;

DI float bf2f(u16 v) { return __uint_as_float(((unsigned)v) << 16); }
DI float bflo(unsigned p) { return __uint_as_float(p << 16); }
DI float bfhi(unsigned p) { return __uint_as_float(p & 0xffff0000u); }
DI unsigned pack_bf16(float a, float b) {
  f32x2 v = {a, b};
  return __builtin_bit_cast(unsigned, __builtin_convertvector(v, bf16x2));
}
DI unsigned pack_f16(float a, float b) {
  f32x2 v = {a, b};
  return __builtin_bit_cast(unsigned, __builtin_convertvector(v, f16x2));
}
DI float rbf(float a) { return bflo(pack_bf16(a, a)); }
template <int CTRL> DI float dpp_add(float v) {
  const int t = __builtin_amdgcn_update_dpp(0, __float_as_int(v), CTRL, 0xf, 0xf, true);
  return v + __int_as_float(t);
}
DI float wave_sum(float v) {
  v = dpp_add<0xB1>(v);
  v = dpp_add<0x4E>(v);
  v = dpp_add<0x141>(v);
  v = dpp_add<0x140>(v);
  auto r = __builtin_amdgcn_permlane16_swap(__float_as_uint(v), __float_as_uint(v), false, false);
  v = __uint_as_float(r[0]) + __uint_as_float(r[1]);
  auto r2 = __builtin_amdgcn_permlane32_swap(__float_as_uint(v), __float_as_uint(v), false, false);
  return __uint_as_float(r2[0]) + __uint_as_float(r2[1]);
}
#define LN_AUX 2
#define WT_AUX 16
DI __amdgpu_buffer_rsrc_t ws_rsrc(char* ws) { return __builtin_amdgcn_make_buffer_rsrc((void*)ws, 0, (int)WS_END, 0x00020000); }
#define KR_AUX 16
#define HT_AUX 16
#define OUT_AUX 0
DI void wst128(__amdgpu_buffer_rsrc_t rs, size_t off, u32x4 v) { __builtin_amdgcn_raw_buffer_store_b128(v, rs, (unsigned)off, 0, WT_AUX); }
DI void wst128k(__amdgpu_buffer_rsrc_t rs, size_t off, u32x4 v) { __builtin_amdgcn_raw_buffer_store_b128(v, rs, (unsigned)off, 0, KR_AUX); }
DI void wst64(__amdgpu_buffer_rsrc_t rs, size_t off, u32x2 v) { __builtin_amdgcn_raw_buffer_store_b64(v, rs, (unsigned)off, 0, HT_AUX); }
DI f32x2 cswz(f32x2 b) { return b; }
DI f32x2 cm_b(f32x2 t, f32x2 a, f32x2 b) {
  f32x2 o;
  asm("v_pk_fma_f32 %0, %1, %2, %3 op_sel:[1,1,0] op_sel_hi:[1,0,1] neg_lo:[0,1,0]" : "=v"(o) : "v"(a), "v"(b), "v"(t));
  return o;
}
DI f32x2 cmul(f32x2 a, f32x2 b, f32x2 bs) {
  const f32x2 t = (f32x2){a.x, a.x} * b;
  return cm_b(t, a, bs);
}
DI f32x2 csq(f32x2 a) { return cmul(a, a, cswz(a)); }
DI f32x2 cexp_p(float ar2, float air, float p) {
  const float mag = __builtin_amdgcn_exp2f(ar2 * p);
  const float ph = __builtin_amdgcn_fractf(air * p);
  return (f32x2){mag * __builtin_amdgcn_cosf(ph), mag * __builtin_amdgcn_sinf(ph)};
}
DI unsigned pk16(f32x2 v) { return __builtin_bit_cast(unsigned, __builtin_convertvector(v, f16x2)); }
DI void pow2_scale(float s, float& scale, float& inv) {
  int ex = (s > 0.f) ? __builtin_amdgcn_frexp_expf(s) : 14;
  ex = min(max(ex, -100), 120);
  scale = ldexpf(1.f, 14 - ex);
  inv = ldexpf(1.f, ex - 14);
}
DI int perm23(int r) { return (r & ~12) | ((r & 4) << 1) | ((r & 8) >> 1); }

constexpr int K1_KGEN = 448, K1_LN = 128, K1_WT = 112;
constexpr int K1_GRID = K1_KGEN + K1_LN + K1_WT;
constexpr int KG_RS = 272;
constexpr int KG_WAVE = 32 * KG_RS;
constexpr int KG_PART = 4 * KG_WAVE;
constexpr int K1_LDS = KG_PART + 4 * 48 * 16 * 4 + 64;

struct KTab { unsigned short v[2048]; };
constexpr KTab make_ktab() {
  KTab t{};
  for (int i = 0; i < 2048; ++i) {
    const int l = 2047 - i;
    int e = 0;
    while ((l >> (8 + e)) != 0) ++e;
    int p = l;
    if (e > 0) {
      const int q = l >> e, rem = l & ((1 << e) - 1), half = 1 << (e - 1);
      p = (q + ((rem > half || (rem == half && (q & 1))) ? 1 : 0)) << e;
    }
    int e2 = 0;
    while ((p >> (8 + e2)) != 0) ++e2;
    if (e2 > 3) e2 = 3;
    const int row = 8 * e2 + ((p >> 7) << (3 - e2)) + ((p & 7) >> e2);
    const int col = (p & 127) >> 3;
    t.v[i] = (unsigned short)(row * 16 + col);
  }
  return t;
}
__device__ const KTab KTAB = make_ktab();

DI void k1_kgen(char* smem, int d, const u16* __restrict__ lar_p, const u16* __restrict__ aim_p,
                const u16* __restrict__ cr_p, const u16* __restrict__ ci_p, const u16* __restrict__ ldt_p,
                char* __restrict__ ws) {
  const int tid = threadIdx.x, lane = tid & 63;
  const int kq = __builtin_amdgcn_readfirstlane(tid >> 6);
  STAMP_BASE(ws, 0); STAMP(0);
  const int idx = d * N_ + tid;
  const float lar = bf2f(lar_p[idx]), aim = bf2f(aim_p[idx]), cr = bf2f(cr_p[idx]), ci = bf2f(ci_p[idx]);
  const float ldt = bf2f(ldt_p[d]);
  const u32x4 tab = *(const u32x4*)(&KTAB.v[tid * 8]);
  const float LOG2E = 1.4426950408889634f;
  const float dt = rbf(__builtin_amdgcn_exp2f(ldt * LOG2E));
  const float are = -rbf(__builtin_amdgcn_exp2f(lar * LOG2E));
  const float ar = are * dt, ai = aim * dt;
  const float Cr = cr * dt, Ci = ci * dt;
  float* red = (float*)(smem + K1_LDS - 64);
  const float sw = wave_sum(2.f * (fabsf(Cr) + fabsf(Ci)));
  float wscale, winv;
  pow2_scale(sw, wscale, winv);
  if (lane == 0) red[kq] = sw;
  STAMP(1);
  const float ar2 = ar * LOG2E, air = ai * 0.15915494309189535f;
  char* wb = smem + kq * KG_WAVE + lane * 4;
  char* hb = wb + 16 * KG_RS;
  const int r16 = lane & 15, gq = lane >> 4;
  const char* base = smem + kq * KG_WAVE + r16 * KG_RS + gq * 16;
  f32x4 acc[3] = {};
  f16x8 bfr[4];
#define CM_A(t, a, b) const f32x2 t = (f32x2){(a).x, (a).x} * (b)
#define CM_B(t, a, bs) cm_b(t, a, bs)
  f32x2 Lo[16], g[8], R[8], z[9];
  const f32x2 E1 = cexp_p(ar2, air, 1.f);
  Lo[1] = cexp_p(ar2, -air, 8.f);
  R[1] = cexp_p(ar2, air, 128.f);
  g[0] = (f32x2){2.f * wscale * Cr, 2.f * wscale * Ci};
  const f32x2 g0s = cswz(g[0]), R1s = cswz(R[1]);
  f32x2 E2, E4, R8, R16;
  {
    const f32x2 E1s = cswz(E1), L1s = cswz(Lo[1]);
    CM_A(t0, E1, E1); CM_A(t1, Lo[1], Lo[1]); CM_A(t2, R[1], R[1]); CM_A(t3, E1, g[0]);
    SCHED_FENCE();
    E2 = CM_B(t0, E1, E1s); Lo[2] = CM_B(t1, Lo[1], L1s); R[2] = CM_B(t2, R[1], R1s); g[1] = CM_B(t3, E1, g0s);
    SCHED_FENCE();
  }
  const f32x2 E2s = cswz(E2), L2s = cswz(Lo[2]), R2s = cswz(R[2]);
  {
    CM_A(t0, E2, E2); CM_A(t1, Lo[2], Lo[2]); CM_A(t2, R[2], R[2]); CM_A(t3, g[0], E2); CM_A(t4, g[1], E2);
    CM_A(t5, Lo[1], Lo[2]); CM_A(t6, R[1], R[2]);
    SCHED_FENCE();
    E4 = CM_B(t0, E2, E2s); Lo[4] = CM_B(t1, Lo[2], L2s); R[4] = CM_B(t2, R[2], R2s); g[2] = CM_B(t3, g[0], E2s);
    g[3] = CM_B(t4, g[1], E2s); Lo[3] = CM_B(t5, Lo[1], L2s); R[3] = CM_B(t6, R[1], R2s);
    SCHED_FENCE();
  }
  const f32x2 E4s = cswz(E4), L4s = cswz(Lo[4]), R4s = cswz(R[4]);
  {
    f32x2 t[12];
#pragma unroll
    for (int k = 0; k < 4; ++k) {
      t[k] = (f32x2){g[k].x, g[k].x} * E4;
      t[4 + k] = (f32x2){Lo[k + 1].x, Lo[k + 1].x} * Lo[4];
      t[8 + k] = (f32x2){R[k + 1].x, R[k + 1].x} * R[4];
    }
    SCHED_FENCE();
#pragma unroll
    for (int k = 0; k < 4; ++k) {
      g[4 + k] = CM_B(t[k], g[k], E4s);
      Lo[5 + k] = CM_B(t[4 + k], Lo[k + 1], L4s);
      if (k < 3) R[5 + k] = CM_B(t[8 + k], R[k + 1], R4s); else R8 = CM_B(t[8 + k], R[4], R4s);
    }
    SCHED_FENCE();
  }
  {
    const f32x2 L8s = cswz(Lo[8]), R8s = cswz(R8), R3s = cswz(R[3]), g4s = cswz(g[4]);
    f32x2 t[33];
#pragma unroll
    for (int k = 1; k < 8; ++k) t[24 + k] = (f32x2){Lo[k].x, Lo[k].x} * Lo[8];
    t[24] = (f32x2){g[0].x, g[0].x} * R8;
    t[32] = (f32x2){R8.x, R8.x} * R8;
#pragma unroll
    for (int c = 0; c < 8; ++c) t[c] = (f32x2){g[c].x, g[c].x} * R[1];
#pragma unroll
    for (int c = 0; c < 4; ++c) { t[8 + c] = (f32x2){g[2 * c].x, g[2 * c].x} * R[2]; t[12 + c] = (f32x2){g[2 * c].x, g[2 * c].x} * R[3]; }
#pragma unroll
    for (int h = 0; h < 4; ++h) { t[16 + 2 * h] = (f32x2){R[4 + h].x, R[4 + h].x} * g[0]; t[17 + 2 * h] = (f32x2){R[4 + h].x, R[4 + h].x} * g[4]; }
    SCHED_FENCE();
    z[0] = CM_B(t[24], g[0], R8s);
    R16 = CM_B(t[32], R8, R8s);
#pragma unroll
    for (int k = 1; k < 8; ++k) Lo[8 + k] = CM_B(t[24 + k], Lo[k], L8s);
#pragma unroll
    for (int c = 0; c < 8; ++c) t[c] = CM_B(t[c], g[c], R1s);
#pragma unroll
    for (int c = 0; c < 4; ++c) { t[8 + c] = CM_B(t[8 + c], g[2 * c], R2s); t[12 + c] = CM_B(t[12 + c], g[2 * c], R3s); }
#pragma unroll
    for (int h = 0; h < 4; ++h) { t[16 + 2 * h] = CM_B(t[16 + 2 * h], R[4 + h], g0s); t[17 + 2 * h] = CM_B(t[17 + 2 * h], R[4 + h], g4s); }
    SCHED_FENCE();
    *(unsigned*)(wb) = 0x00003c00u;
#pragma unroll
    for (int k = 1; k < 16; ++k) *(unsigned*)(wb + k * KG_RS) = pk16(Lo[k]);
#pragma unroll
    for (int c = 0; c < 8; ++c) *(unsigned*)(hb + c * KG_RS) = pk16(g[c]);
#pragma unroll
    for (int i = 0; i < 8; ++i) *(unsigned*)(hb + (8 + i) * KG_RS) = pk16(t[i]);
#pragma unroll
    for (int ks = 0; ks < 4; ++ks) {
      bfr[ks] = *(const f16x8*)(base + ks * 64);
      acc[0] = __builtin_amdgcn_mfma_f32_16x16x32_f16(*(const f16x8*)(base + 16 * KG_RS + ks * 64), bfr[ks], acc[0], 0, 0, 0);
    }
#pragma unroll
    for (int i = 0; i < 16; ++i) *(unsigned*)(hb + i * KG_RS) = pk16(t[8 + i]);
#pragma unroll
    for (int ks = 0; ks < 4; ++ks)
      acc[1] = __builtin_amdgcn_mfma_f32_16x16x32_f16(*(const f16x8*)(base + 16 * KG_RS + ks * 64), bfr[ks], acc[1], 0, 0, 0);
    SCHED_FENCE();
  }
  {
    const f32x2 z0s = cswz(z[0]);
    f32x2 t[9];
#pragma unroll
    for (int k = 1; k < 8; ++k) t[k] = (f32x2){R[k].x, R[k].x} * z[0];
    t[8] = (f32x2){R16.x, R16.x} * g[0];
    SCHED_FENCE();
#pragma unroll
    for (int k = 1; k < 8; ++k) z[k] = CM_B(t[k], R[k], z0s);
    z[8] = CM_B(t[8], R16, g0s);
    SCHED_FENCE();
#pragma unroll
    for (int h = 0; h < 9; ++h) *(unsigned*)(hb + h * KG_RS) = pk16(z[h]);
#pragma unroll
    for (int ks = 0; ks < 4; ++ks)
      acc[2] = __builtin_amdgcn_mfma_f32_16x16x32_f16(*(const f16x8*)(base + 16 * KG_RS + ks * 64), bfr[ks], acc[2], 0, 0, 0);
  }
  STAMP(2);
  {
    float* part = (float*)(smem + KG_PART) + kq;
#pragma unroll
    for (int m = 0; m < 3; ++m)
#pragma unroll
      for (int j = 0; j < 4; ++j) part[((16 * m + 4 * gq + j) * 16 + r16) * 4] = acc[m][j] * winv;
  }
  STAMP(3);
  __syncthreads();
  STAMP(4);
  {
    float scale, inv_scale;
    pow2_scale((red[0] + red[1]) + (red[2] + red[3]), scale, inv_scale);
    const f32x4* part = (const f32x4*)(smem + KG_PART);
    unsigned pk[4];
#pragma unroll
    for (int j = 0; j < 4; ++j) {
      const f32x4 p0 = part[tab[j] & 0xffffu], p1 = part[tab[j] >> 16];
      pk[j] = pack_f16(((p0[0] + p0[1]) + (p0[2] + p0[3])) * scale, ((p1[0] + p1[1]) + (p1[2] + p1[3])) * scale);
    }
    const __amdgpu_buffer_rsrc_t rs = ws_rsrc(ws);
    const size_t kr = OFF_KR + (size_t)d * KR_LEN * 2;
    wst128k(rs, kr + tid * 16, (u32x4){pk[0], pk[1], pk[2], pk[3]});
    if (tid < 32) wst128k(rs, kr + 4096 + tid * 16, (u32x4){0u, 0u, 0u, 0u});
    if (tid == 0) ((float*)(ws + OFF_KS))[d] = inv_scale;
  }
  STAMP(5); STAMPW(14);
}

DI float reduce4(const float (&a)[4]) {
  float v[2];
#pragma unroll
  for (int i = 0; i < 2; ++i) {
    auto r = __builtin_amdgcn_permlane32_swap(__float_as_uint(a[i]), __float_as_uint(a[i + 2]), false, false);
    v[i] = __uint_as_float(r[0]) + __uint_as_float(r[1]);
  }
  auto r = __builtin_amdgcn_permlane16_swap(__float_as_uint(v[0]), __float_as_uint(v[1]), false, false);
  float x = __uint_as_float(r[0]) + __uint_as_float(r[1]);
  x = dpp_add<0xB1>(x);
  x = dpp_add<0x4E>(x);
  x = dpp_add<0x141>(x);
  x = dpp_add<0x140>(x);
  return x;
}
DI float bcast_row(float w, int t) { return __uint_as_float(__builtin_amdgcn_readlane(__float_as_uint(w), 16 * t)); }

constexpr int LN_PL = 448 * 32 + 56 * 16;
DI void k1_ln(char* smem, int blk, const u16* __restrict__ x, char* __restrict__ ws) {
  const int tid = threadIdx.x, lane = tid & 63, wv = tid >> 6;
  STAMP_BASE(ws, 0); STAMP(0);
  const int tok0 = blk * 16 + wv * 4;
  const bool act = lane < 56;
  u32x4 xr[4];
#pragma unroll
  for (int t = 0; t < 4; ++t) {
    xr[t] = (u32x4){0u, 0u, 0u, 0u};
    if (act) xr[t] = *(const u32x4*)(x + (size_t)(tok0 + t) * D_ + lane * 8);
  }
  f32x2 xf[4][4];
  float part[4];
#pragma unroll
  for (int t = 0; t < 4; ++t) {
#pragma unroll
    for (int j = 0; j < 4; ++j) xf[t][j] = (f32x2){bflo(xr[t][j]), bfhi(xr[t][j])};
    const f32x2 s2 = (xf[t][0] + xf[t][1]) + (xf[t][2] + xf[t][3]);
    part[t] = s2.x + s2.y;
  }
  const float wm = reduce4(part) * (1.f / D_);
#pragma unroll
  for (int t = 0; t < 4; ++t) {
    const float m = bcast_row(wm, t);
    f32x2 q2 = {0.f, 0.f};
#pragma unroll
    for (int j = 0; j < 4; ++j) { const f32x2 dd = xf[t][j] - (f32x2){m, m}; q2 = __builtin_elementwise_fma(dd, dd, q2); }
    part[t] = act ? q2.x + q2.y : 0.f;
  }
  const float wq = reduce4(part);
  const float sd = rbf(sqrtf(rbf(rbf(wq * (1.f / D_)) + 1e-5f)));
  const float wr = 1.f / sd;
  const float wb = rbf(wm);
  STAMP(1);
  f32x2 un[4][4];
#pragma unroll
  for (int t = 0; t < 4; ++t) {
    const float mb = bcast_row(wb, t), ri = bcast_row(wr, t);
#pragma unroll
    for (int j = 0; j < 4; ++j) {
      const f32x2 dd = xf[t][j] - (f32x2){mb, mb};
      const unsigned pb = pack_bf16(dd.x, dd.y);
      un[t][j] = (f32x2){bflo(pb), bfhi(pb)} * (f32x2){ri, ri};
    }
  }
  if (act) {
    char* ub = smem + lane * (8 * 32 + 16) + wv * 8;
#pragma unroll
    for (int j = 0; j < 8; ++j) {
      u32x2 pu, px;
#pragma unroll
      for (int t = 0; t < 2; ++t) {
        pu[t] = pack_bf16(un[2 * t][j >> 1][j & 1], un[2 * t + 1][j >> 1][j & 1]);
        const unsigned a = xr[2 * t][j >> 1], b = xr[2 * t + 1][j >> 1];
        px[t] = (j & 1) ? __builtin_amdgcn_perm(b, a, 0x07060302u) : __builtin_amdgcn_perm(b, a, 0x05040100u);
      }
      *(u32x2*)(ub + j * 32) = pu;
      *(u32x2*)(ub + LN_PL + j * 32) = px;
    }
  }
  STAMP(2);
  __syncthreads();
  STAMP(3);
  const __amdgpu_buffer_rsrc_t wrs = ws_rsrc(ws);
#pragma unroll
  for (int i = 0; i < 4; ++i) {
    const int ch = i * 128 + (tid >> 1), v = tid & 1;
    if (ch < D_) {
      const int o = ch * 32 + (ch >> 3) * 16 + v * 16;
      const u32x4 a = *(const u32x4*)(smem + o);
      const u32x4 b = *(const u32x4*)(smem + LN_PL + o);
      __builtin_amdgcn_raw_buffer_store_b128(a, wrs, (unsigned)(OFF_UT + ((size_t)ch * L_ + blk * 16 + v * 8) * 2), 0, LN_AUX);
      __builtin_amdgcn_raw_buffer_store_b128(b, wrs, (unsigned)(OFF_XT + ((size_t)ch * L_ + blk * 16 + v * 8) * 2), 0, LN_AUX);
    }
  }
  STAMP(4); STAMPW(14);
}

typedef __attribute__((address_space(3))) bf16x4* lds_bf16x4_p;
DI void k1_wtile(char* smem, const u16* __restrict__ src, int ldn, size_t dsto, const u16* __restrict__ b1s, char* ws) {
  const int tid = threadIdx.x, lane = tid & 63, wv = tid >> 6;
  const __amdgpu_buffer_rsrc_t rs = ws_rsrc(ws);
  STAMP_BASE(ws, 0); STAMP(0);
  u32x4 v[7];
#pragma unroll
  for (int i = 0; i < 7; ++i) {
    const int c = i * 256 + tid, k = c >> 2, q = c & 3;
    v[i] = *(const u32x4*)(src + (size_t)k * ldn + q * 8);
  }
  unsigned b1v = 0;
  if (b1s) b1v = b1s[perm23(tid & 31)];
  SCHED_FENCE();
  {
    const int q = tid & 3;
    char* dst = smem + (tid >> 2) * 64 + 32 * (q >> 1) + 8 * (q & 1);
#pragma unroll
    for (int i = 0; i < 7; ++i) {
      *(u32x2*)(dst + i * 4096) = (u32x2){v[i][0], v[i][1]};
      *(u32x2*)(dst + i * 4096 + 16) = (u32x2){v[i][2], v[i][3]};
    }
  }
  __syncthreads();
  STAMP(1);
  {
    const int hh = lane >> 5, blk = (lane >> 4) & 1, q = (lane & 15) >> 2, p = lane & 3;
    const char* rd = smem + (8 * hh + q) * 64 + 32 * blk + 8 * p + wv * 1024;
#pragma unroll
    for (int i = 0; i < 7; ++i) {
      const bf16x4 lo = __builtin_amdgcn_ds_read_tr16_b64_v4bf16((lds_bf16x4_p)(rd + i * 4096));
      const bf16x4 hi = __builtin_amdgcn_ds_read_tr16_b64_v4bf16((lds_bf16x4_p)(rd + i * 4096 + 256));
      const bf16x8 f = __builtin_shufflevector(lo, hi, 0, 1, 2, 3, 4, 5, 6, 7);
      wst128(rs, dsto + (size_t)((i * 4 + wv) * 64 + lane) * 16, __builtin_bit_cast(u32x4, f));
    }
  }
  STAMP(2);
  if (b1s) {
    const int cp = tid & 15, kg = tid >> 4;
    f32x2 sacc = {0.f, 0.f};
#pragma unroll
    for (int k = 0; k < 28; ++k) {
      const unsigned w = *(const unsigned*)(smem + (kg * 28 + k) * 64 + cp * 4);
      sacc += (f32x2){bflo(w), bfhi(w)};
    }
    f32x2* red = (f32x2*)(smem + 448 * 64);
    red[tid] = sacc;
    __syncthreads();
    if (tid < 64) {
      const float* redf = (const float*)red;
      float a = 0.f;
#pragma unroll
      for (int g = 0; g < 16; ++g) a += redf[g * 32 + (tid & 31)];
      const float hi = rbf(a);
      const unsigned w0 = pack_bf16(hi, a - hi);
      wst128(rs, dsto + (size_t)(28 * 64 + tid) * 16, (u32x4){tid < 32 ? w0 : 0u, tid < 32 ? b1v : 0u, 0u, 0u});
    }
  }
  STAMP(3); STAMPW(14);
}

__global__ __launch_bounds__(256, 3) void k1_kernel(const u16* __restrict__ x, const u16* __restrict__ lar,
                                                    const u16* __restrict__ aim, const u16* __restrict__ cr,
                                                    const u16* __restrict__ ci, const u16* __restrict__ ldt,
                                                    const u16* __restrict__ w1, const u16* __restrict__ b1,
                                                    const u16* __restrict__ w2, char* __restrict__ ws) {
  __shared__ __attribute__((aligned(16))) char smem[K1_LDS];
  int b = blockIdx.x;
  if (b < K1_LN) {
    if (b == 0 && threadIdx.x < 65) ((unsigned*)(ws + OFF_CNT))[threadIdx.x * 64] = 0u;
    k1_ln(smem, b, x, ws);
    return;
  }
  b -= K1_LN;
  if (b < K1_KGEN) { k1_kgen(smem, b, lar, aim, cr, ci, ldt, ws); return; }
  b -= K1_KGEN;
  if (b < 56) {
    k1_wtile(smem, w1 + b * 32, F_, OFF_W1 + (size_t)b * W1_FRAGS * 1024, b1 + b * 32, ws);
  } else {
    const int dt = (b - 56) >> 2, kc = (b - 56) & 3;
    k1_wtile(smem, w2 + (size_t)kc * 448 * D_ + dt * 32, D_, OFF_W2 + ((size_t)dt * 112 + kc * 28) * 1024, nullptr, ws);
  }
}

constexpr int CV_CS = 4160;
constexpr int CV_KOFF = 8 * CV_CS;
constexpr int CV_CH = CV_KOFF + KR_LEN * 2;
constexpr int K3_TILE = 448 * 128;
constexpr int K3_RED = 8 * 7 * 2048;
constexpr int K3_STAT = K3_RED;
constexpr int K3_LDS = K3_STAT + 4 * 64 * 8;
static_assert(2 * CV_CH <= K3_LDS && 8 * 3 * 1024 <= CV_CH, "lds");
typedef __attribute__((address_space(1))) unsigned gu32;

DI bf16x8 k3_bfrag(const char* smem, unsigned lane_off0, unsigned lane_off1, int off) {
  const bf16x4 lo = __builtin_amdgcn_ds_read_tr16_b64_v4bf16((lds_bf16x4_p)(smem + lane_off0 + off));
  const bf16x4 hi = __builtin_amdgcn_ds_read_tr16_b64_v4bf16((lds_bf16x4_p)(smem + lane_off1 + off));
  return __builtin_shufflevector(lo, hi, 0, 1, 2, 3, 4, 5, 6, 7);
}
#define W1_EARLY 5
#define H1_AUX 16
#define POLL_SLEEP 1
#define H1_WIN 6
template <int N> DI void wait_vm() { asm volatile("s_waitcnt vmcnt(%0)" ::"i"(N) : "memory"); }

__global__ __launch_bounds__(512, 2) void k2_kernel(const u16* __restrict__ dskip, const u16* __restrict__ b2,
                                                    char* __restrict__ ws, u16* __restrict__ out) {
  __shared__ __attribute__((aligned(16))) char smem[K3_LDS];
  const int tid = threadIdx.x, lane = tid & 63;
  const int wv = __builtin_amdgcn_readfirstlane(tid >> 6);
  const int xcd = blockIdx.x & 7, idx = blockIdx.x >> 3;
  const int grp = 4 * xcd + idx / 7, j = idx % 7;
  const int tok0 = grp * 64, ft = j * 8 + wv;
  const int sh = (wv >> 1) & 1;
  const int s2 = wv >> 2;
  STAMP_BASE(ws, 2); STAMP(0);
  const __amdgpu_buffer_rsrc_t wrs = ws_rsrc(ws);
  const bf16x8* w1f = (const bf16x8*)(ws + OFF_W1) + (size_t)ft * W1_FRAGS * 64 + lane;
  const bf16x8* w1a = w1f + sh * 128;
  const bf16x8* w1b = w1f + (1 - sh) * 128;
  bf16x8 A[28];
#define ISSUEA(c) do { A[4 * (c)] = w1a[(4 * (c)) * 64]; A[4 * (c) + 1] = w1a[(4 * (c) + 1) * 64]; \
    A[4 * (c) + 2] = w1b[(4 * (c)) * 64]; A[4 * (c) + 3] = w1b[(4 * (c) + 1) * 64]; } while (0)
#define LDA(n) ((((n) & 2) == 0) ? w1a[(4 * ((n) >> 2) + ((n) & 1)) * 64] : w1b[(4 * ((n) >> 2) + ((n) & 1)) * 64])
  bf16x8 Aext;
  {
    const int d = 2 * blockIdx.x + (wv >> 2), cw = wv & 3, m0 = tid & 255;
    char* cs = smem + (wv >> 2) * CV_CH;
    const u16* uT = (const u16*)(ws + OFF_UT) + (size_t)d * L_;
    const u16* xT = (const u16*)(ws + OFF_XT) + (size_t)d * L_;
    const char* kr = ws + OFF_KR + (size_t)d * KR_LEN * 2;
    u32x4 c0 = {0u, 0u, 0u, 0u};
    if (m0 > 0) c0 = *(const u32x4*)(uT + (m0 - 1) * 8);
    const u32x4 c1 = *(const u32x4*)(uT + m0 * 8);
    const u32x4 k0 = *(const u32x4*)(kr + m0 * 16);
    u32x4 k1 = {0u, 0u, 0u, 0u};
    if (m0 < 32) k1 = *(const u32x4*)(kr + 4096 + m0 * 16);
    const int c16 = lane & 15, g4 = lane >> 4;
    u32x2 ue[2], xe[2];
#pragma unroll
    for (int i = 0; i < 2; ++i) {
      const int pp = 256 * (2 * cw + i) + 16 * c16 + 4 * g4;
      ue[i] = *(const u32x2*)(uT + pp);
      xe[i] = *(const u32x2*)(xT + pp);
    }
    const float inv_scale = ((const float*)(ws + OFF_KS))[d];
    const float dsk = bf2f(dskip[d]);
    SCHED_FENCE();
    *(u32x4*)(cs + CV_KOFF + m0 * 16) = k0;
    if (m0 < 32) *(u32x4*)(cs + CV_KOFF + 4096 + m0 * 16) = k1;
    if (m0 < 8) *(u32x4*)(cs + m0 * CV_CS) = (u32x4){0u, 0u, 0u, 0u};
    {
      unsigned pq[8];
#pragma unroll
      for (int e = 0; e < 4; ++e) {
        pq[e] = pack_f16(bflo(c0[e]), bfhi(c0[e]));
        pq[4 + e] = pack_f16(bflo(c1[e]), bfhi(c1[e]));
      }
      unsigned qq[7];
#pragma unroll
      for (int i = 0; i < 7; ++i) qq[i] = __builtin_amdgcn_alignbit(pq[i + 1], pq[i], 16);
      char* dst = cs + (m0 + 1) * 16;
#pragma unroll
      for (int rho = 0; rho < 8; ++rho) {
        const int k = (rho + 1) >> 1;
        const u32x4 w = (rho & 1) ? (u32x4){pq[k], pq[k + 1], pq[k + 2], pq[k + 3]} : (u32x4){qq[k], qq[k + 1], qq[k + 2], qq[k + 3]};
        *(u32x4*)(dst + rho * CV_CS) = w;
      }
    }
    __syncthreads();
    STAMP(9);
    f32x4 cacc[8] = {};
    const char* abase = cs + (c16 & 7) * CV_CS + ((c16 & 8) + 8 * g4) * 2;
    const char* bbase = cs + CV_KOFF + (2032 - 16 * c16 + 8 * g4) * 2;
#pragma unroll
    for (int si = 0; si < 2; ++si) {
      const int sg = 2 * cw + si;
      f16x8 CA[8], CB[8];
#pragma unroll
      for (int i = 0; i < 8; ++i) {
        CA[i] = *(const f16x8*)(abase + sg * 64 + i * 512);
        CB[i] = *(const f16x8*)(bbase + sg * 64 - i * 512);
      }
#pragma unroll
      for (int t = 0; t < 8; ++t) {
#pragma unroll
        for (int ia = 0; ia <= t; ++ia)
          cacc[t] = __builtin_amdgcn_mfma_f32_16x16x32_f16(CA[ia], CB[t - ia], cacc[t], 0, 0, 0);
        if (t >= 3) {
          const int n0 = (si * 5 + (t - 3)) * 2;
          if (n0 < 4 * W1_EARLY) { A[n0] = LDA(n0); A[n0 + 1] = LDA(n0 + 1); }
          SCHED_FENCE();
        }
      }
      SCHED_FENCE();
    }
    __syncthreads();
    f32x4* red = (f32x4*)cs;
    f32x4 own[2] = {};
#pragma unroll
    for (int t = 0; t < 8; ++t) {
      if ((t >> 1) == cw) own[t & 1] = cacc[t];
      else red[(t * 3 + (cw > (t >> 1) ? cw - 1 : cw)) * 64 + lane] = cacc[t];
    }
    __syncthreads();
#pragma unroll
    for (int i = 0; i < 2; ++i) {
      const int t = 2 * cw + i;
      const f32x4 sum = (own[i] + red[(t * 3) * 64 + lane]) + (red[(t * 3 + 1) * 64 + lane] + red[(t * 3 + 2) * 64 + lane]);
      float h[4];
#pragma unroll
      for (int e = 0; e < 4; ++e) {
        const float uu = (e & 1) ? bfhi(ue[i][e >> 1]) : bflo(ue[i][e >> 1]);
        const float xx = (e & 1) ? bfhi(xe[i][e >> 1]) : bflo(xe[i][e >> 1]);
        const float y = rbf(sum[e] * inv_scale);
        const float tt = rbf(y + rbf(uu * dsk));
        h[e] = xx + tt;
      }
      __builtin_amdgcn_raw_buffer_store_b64((u32x2){pack_bf16(h[0], h[1]), pack_bf16(h[2], h[3])}, wrs,
                                            (unsigned)(OFF_HT + ((size_t)d * HT_LD + 256 * t + 16 * c16 + 4 * g4) * 2), 0, 16);
    }
  }
  STAMP(10);
  wait_vm<0>();
  __builtin_amdgcn_s_barrier();
  asm volatile("" ::: "memory");
  STAMP(11);
  if (wv == 0) {
    gu32* gc = (gu32*)(ws + OFF_CNT) + 32 * 64;
    unsigned last = 0;
    if (lane == 0) {
      if (__hip_atomic_fetch_add(gc + (blockIdx.x & 15) * 64, 1u, __ATOMIC_RELAXED, __HIP_MEMORY_SCOPE_AGENT) == 13u)
        last = __hip_atomic_fetch_add(gc + 16 * 64, 1u, __ATOMIC_RELAXED, __HIP_MEMORY_SCOPE_AGENT) == 15u;
    }
    if (__builtin_amdgcn_readfirstlane(last)) {
      if (lane < 16) __hip_atomic_store(gc + (17 + lane) * 64, 1u, __ATOMIC_RELAXED, __HIP_MEMORY_SCOPE_AGENT);
    } else if (lane == 0) {
      for (unsigned spins = 0; spins < (1u << 20); ++spins) {
        if (__hip_atomic_load(gc + (17 + (blockIdx.x & 15)) * 64, __ATOMIC_RELAXED, __HIP_MEMORY_SCOPE_AGENT) != 0u) break;
        __builtin_amdgcn_s_sleep(POLL_SLEEP);
      }
    }
    __builtin_amdgcn_fence(__ATOMIC_ACQUIRE, "wavefront");
  }
  SCHED_FENCE();
#pragma unroll
  for (int c = W1_EARLY; c < 7; ++c) ISSUEA(c);
  Aext = w1f[28 * 64];
  SCHED_FENCE();
  __builtin_amdgcn_s_barrier();
  __builtin_amdgcn_fence(__ATOMIC_ACQUIRE, "wavefront");
  asm volatile("" ::: "memory");
  STAMP(12);
  const unsigned hsrc = (unsigned)(OFF_HT + ((size_t)(tid >> 3) * HT_LD + tok0 + (tid & 7) * 8) * 2);
  const __amdgpu_buffer_rsrc_t w2rs = __builtin_amdgcn_make_buffer_rsrc((void*)(ws + OFF_W2), 0, D_ * F_ * 2, 0x00020000);
  const unsigned w2v = ((2 * j) * 112 * 64 + lane) * 16;
#define KSLOT(sl) ((sl) < 2 ? 16 * j + 2 * wv + (sl) : (12 * wv + (sl) - 2) + ((12 * wv + (sl) - 2) >= 16 * j ? 16 : 0))
#define LDW2(sl, dt) __builtin_bit_cast(bf16x8, __builtin_amdgcn_raw_buffer_load_b128(w2rs, w2v + (dt) * 112 * 1024, KSLOT(sl) * 1024, 0))
  u32x4 st[7];
  bf16x8 a0[14], a1[14];
#define ISSUE2(c) do { a0[2 * (c)] = LDW2(2 * (c), 0); a1[2 * (c)] = LDW2(2 * (c), 1); \
    a0[2 * (c) + 1] = LDW2(2 * (c) + 1, 0); a1[2 * (c) + 1] = LDW2(2 * (c) + 1, 1); } while (0)
#pragma unroll
  for (int c = 0; c < 7; ++c) st[c] = __builtin_amdgcn_raw_buffer_load_b128(wrs, hsrc, 64 * c * HT_LD * 2, 16);
  SCHED_FENCE();
  const int hh = lane >> 5, col = lane & 31;
  const unsigned q = (lane & 15) >> 2, p = lane & 3, blk = (lane >> 4) & 1;
  char* lw = smem + 128 * (tid >> 3) + 16 * ((tid & 7) ^ (((tid >> 4) & 1) << 2));
  unsigned oa[2][2], ob[2][2];
  {
    const unsigned r0 = 8 * hh + q, r1 = 8 * hh + 4 + q, sw = ((q >> 1) & 1) << 2;
#pragma unroll
    for (int it = 0; it < 2; ++it) {
      const unsigned c = (wv & 1) ^ it;
      const unsigned chk = (4 * c + 2 * blk + (p >> 1)) ^ sw;
      const unsigned x0 = 128 * r0 + 16 * chk + 8 * (p & 1), x1 = 128 * r1 + 16 * chk + 8 * (p & 1);
      oa[it][0] = x0 + sh * 4096; oa[it][1] = x1 + sh * 4096;
      ob[it][0] = x0 + (1 - sh) * 4096; ob[it][1] = x1 + (1 - sh) * 4096;
    }
  }
  f32x16 acc[2] = {};
  float ssum = 0.f, ssq = 0.f;
  const bf16x2 ones2 = {(__bf16)1.0f, (__bf16)1.0f};
#define WRITE1(c) do { *(u32x4*)(lw + (c) * 8192) = st[c]; } while (0)
#define RDB(n, it) ((((n) & 2) == 0) ? k3_bfrag(smem, oa[it][0], oa[it][1], ((n) >> 2) * 8192 + ((n) & 1) * 2048) \
                                      : k3_bfrag(smem, ob[it][0], ob[it][1], ((n) >> 2) * 8192 + ((n) & 1) * 2048))
  bf16x8 Bf[3][2];
#pragma unroll
  for (int c = 0; c < 7; ++c) WRITE1(c);
  __syncthreads();
  STAMP(1);
  Bf[0][0] = RDB(0, 0); Bf[0][1] = RDB(0, 1); Bf[1][0] = RDB(1, 0); Bf[1][1] = RDB(1, 1);
#pragma unroll
  for (int n = 0; n < 28; ++n) {
    const int c = n >> 2, i = n & 3;
    acc[0] = __builtin_amdgcn_mfma_f32_32x32x16_bf16(A[n], Bf[n % 3][0], acc[0], 0, 0, 0);
    if (n + 2 < 28) Bf[(n + 2) % 3][0] = RDB(n + 2, 0);
    if (i < 2) {
      const u32x4 B = __builtin_bit_cast(u32x4, Bf[n % 3][0]);
      const bf16x2 pr = __builtin_bit_cast(bf16x2, s2 ? B[2] : B[0]);
      ssum = __builtin_amdgcn_fdot2_f32_bf16(pr, ones2, ssum, false);
      ssq = __builtin_amdgcn_fdot2_f32_bf16(pr, pr, ssq, false);
    }
    SCHED_FENCE();
    acc[1] = __builtin_amdgcn_mfma_f32_32x32x16_bf16(A[n], Bf[n % 3][1], acc[1], 0, 0, 0);
    if (n + 2 < 28) Bf[(n + 2) % 3][1] = RDB(n + 2, 1);
    if (i < 2) {
      const u32x4 B = __builtin_bit_cast(u32x4, Bf[n % 3][0]);
      const bf16x2 pr = __builtin_bit_cast(bf16x2, s2 ? B[3] : B[1]);
      ssum = __builtin_amdgcn_fdot2_f32_bf16(pr, ones2, ssum, false);
      ssq = __builtin_amdgcn_fdot2_f32_bf16(pr, pr, ssq, false);
    }
    if (i == 3) ISSUE2(c);
    SCHED_FENCE();
  }
  STAMP(2);
  float2* stats = (float2*)(smem + K3_STAT);
  {
    auto r1 = __builtin_amdgcn_permlane32_swap(__float_as_uint(ssum), __float_as_uint(ssum), false, false);
    ssum = __uint_as_float(r1[0]) + __uint_as_float(r1[1]);
    auto r2 = __builtin_amdgcn_permlane32_swap(__float_as_uint(ssq), __float_as_uint(ssq), false, false);
    ssq = __uint_as_float(r2[0]) + __uint_as_float(r2[1]);
    if (hh == 0) stats[(wv >> 1) * 64 + (wv & 1) * 32 + col] = make_float2(ssum, ssq);
  }
  const int t2 = wv >> 1, hf2 = wv & 1;
  const int c2 = t2 & 1;
  bf16x8 res;
  {
    const unsigned r0 = 8 * hh + q, r1 = 8 * hh + 4 + q, sw = ((q >> 1) & 1) << 2;
    const unsigned chk = (4 * c2 + 2 * blk + (p >> 1)) ^ sw;
    res = k3_bfrag(smem, 128 * r0 + 16 * chk + 8 * (p & 1), 128 * r1 + 16 * chk + 8 * (p & 1), (j * 4 + (t2 >> 1) * 2 + hf2) * 2048);
  }
  __syncthreads();
  float sdv[2], c1v[2], c2v[2];
#pragma unroll
  for (int it = 0; it < 2; ++it) {
    const unsigned c = (wv & 1) ^ it;
    const float2 s0 = stats[c * 32 + col], s1 = stats[64 + c * 32 + col], s2_ = stats[128 + c * 32 + col], s3 = stats[192 + c * 32 + col];
    const float mean = ((s0.x + s1.x) + (s2_.x + s3.x)) * (1.f / D_);
    const float var = fmaxf(__builtin_fmaf(-mean, mean, ((s0.y + s1.y) + (s2_.y + s3.y)) * (1.f / D_)), 0.f);
    const float sd = rbf(sqrtf(rbf(rbf(var) + 1e-5f)));
    const float rs = __builtin_amdgcn_rcpf(sd);
    sdv[it] = sd;
    c1v[it] = rs * -2.3022082f;
    c2v[it] = rs * rs * rs * -0.10294324f;
    const unsigned w0 = pack_bf16(-mean, -mean), w1 = pack_bf16(sd, 0.f);
    const u32x4 bw = {hh ? 0u : w0, hh ? 0u : w1, 0u, 0u};
    acc[it] = __builtin_amdgcn_mfma_f32_32x32x16_bf16(Aext, __builtin_bit_cast(bf16x8, bw), acc[it], 0, 0, 0);
  }
  const __amdgpu_buffer_rsrc_t h1rs = __builtin_amdgcn_make_buffer_rsrc((void*)(ws + OFF_H1), 0, L_ * F_ * 2, 0x00020000);
  u32x4 own[2][2];
#pragma unroll
  for (int it = 0; it < 2; ++it) {
    const unsigned c = (wv & 1) ^ it;
    const unsigned fb = (((grp * 2 + c) * 112 + 2 * ft) * 64 + lane) * 16;
    const f32x2 c1p = {c1v[it], c1v[it]}, c2p = {c2v[it], c2v[it]}, sdp = {sdv[it], sdv[it]};
#pragma unroll
    for (int hf = 0; hf < 2; ++hf) {
      f32x2 a[4], z[4];
#pragma unroll
      for (int e = 0; e < 4; ++e) a[e] = (f32x2){acc[it][8 * hf + 2 * e], acc[it][8 * hf + 2 * e + 1]};
#pragma unroll
      for (int e = 0; e < 4; ++e) z[e] = a[e] * a[e];
#pragma unroll
      for (int e = 0; e < 4; ++e) z[e] = __builtin_elementwise_fma(z[e], c2p, c1p);
#pragma unroll
      for (int e = 0; e < 4; ++e) z[e] = a[e] * z[e];
#pragma unroll
      for (int e = 0; e < 4; ++e) z[e] = (f32x2){__builtin_amdgcn_exp2f(z[e].x), __builtin_amdgcn_exp2f(z[e].y)};
#pragma unroll
      for (int e = 0; e < 4; ++e) z[e] = __builtin_elementwise_fma(z[e], sdp, sdp);
#pragma unroll
      for (int e = 0; e < 4; ++e) z[e] = (f32x2){__builtin_amdgcn_rcpf(z[e].x), __builtin_amdgcn_rcpf(z[e].y)};
#pragma unroll
      for (int e = 0; e < 4; ++e) z[e] = a[e] * z[e];
      const u32x4 pk = {pack_bf16(z[0].x, z[0].y), pack_bf16(z[1].x, z[1].y), pack_bf16(z[2].x, z[2].y), pack_bf16(z[3].x, z[3].y)};
      __builtin_amdgcn_raw_buffer_store_b128(pk, h1rs, fb + hf * 1024, 0, 16);
      own[it][hf] = pk;
      SCHED_FENCE();
    }
  }
  f32x16 acc2[4] = {};
#pragma unroll
  for (int ks = 0; ks < 2; ++ks) {
    const bf16x8 y0 = __builtin_bit_cast(bf16x8, own[0][ks]), y1 = __builtin_bit_cast(bf16x8, own[1][ks]);
    acc2[0] = __builtin_amdgcn_mfma_f32_32x32x16_bf16(a0[ks], y0, acc2[0], 0, 0, 0);
    acc2[1] = __builtin_amdgcn_mfma_f32_32x32x16_bf16(a0[ks], y1, acc2[1], 0, 0, 0);
    acc2[2] = __builtin_amdgcn_mfma_f32_32x32x16_bf16(a1[ks], y0, acc2[2], 0, 0, 0);
    acc2[3] = __builtin_amdgcn_mfma_f32_32x32x16_bf16(a1[ks], y1, acc2[3], 0, 0, 0);
  }
  STAMP(3);
  wait_vm<0>();
  __builtin_amdgcn_s_barrier();
  asm volatile("" ::: "memory");
  STAMP(4);
  gu32* cnt = (gu32*)(ws + OFF_CNT) + grp * 64;
  if (wv == 0) {
    if (lane == 0) {
      __hip_atomic_fetch_add(cnt, 1u, __ATOMIC_RELAXED, __HIP_MEMORY_SCOPE_AGENT);
      for (unsigned spins = 0; spins < (1u << 20); ++spins) {
        if (__hip_atomic_load(cnt, __ATOMIC_RELAXED, __HIP_MEMORY_SCOPE_AGENT) >= 7u) break;
        __builtin_amdgcn_s_sleep(1);
      }
    }
    __builtin_amdgcn_fence(__ATOMIC_ACQUIRE, "wavefront");
  }
  __builtin_amdgcn_s_barrier();
  __builtin_amdgcn_fence(__ATOMIC_ACQUIRE, "wavefront");
  asm volatile("" ::: "memory");
  STAMP(5);
  const unsigned hb0 = (((grp * 2 + (wv & 1)) * 112) * 64 + lane) * 16, hb1 = (((grp * 2 + 1 - (wv & 1)) * 112) * 64 + lane) * 16;
  u32x4 b0[H1_WIN], b1v[H1_WIN];
#pragma unroll
  for (int i = 0; i < H1_WIN; ++i) {
    b0[i] = __builtin_amdgcn_raw_buffer_load_b128(h1rs, hb0, KSLOT(2 + i) * 1024, H1_AUX);
    b1v[i] = __builtin_amdgcn_raw_buffer_load_b128(h1rs, hb1, KSLOT(2 + i) * 1024, H1_AUX);
  }
  const int d0 = j * 64 + (t2 >> 1) * 32 + 16 * hf2 + 8 * hh;
  const u32x4 bz = *(const u32x4*)(b2 + d0);
  SCHED_FENCE();
#pragma unroll
  for (int ks = 0; ks < 12; ++ks) {
    const int sl = ks % H1_WIN;
    const bf16x8 y0 = __builtin_bit_cast(bf16x8, b0[sl]), y1 = __builtin_bit_cast(bf16x8, b1v[sl]);
    acc2[0] = __builtin_amdgcn_mfma_f32_32x32x16_bf16(a0[2 + ks], y0, acc2[0], 0, 0, 0);
    acc2[1] = __builtin_amdgcn_mfma_f32_32x32x16_bf16(a0[2 + ks], y1, acc2[1], 0, 0, 0);
    acc2[2] = __builtin_amdgcn_mfma_f32_32x32x16_bf16(a1[2 + ks], y0, acc2[2], 0, 0, 0);
    acc2[3] = __builtin_amdgcn_mfma_f32_32x32x16_bf16(a1[2 + ks], y1, acc2[3], 0, 0, 0);
    if (ks + H1_WIN < 12) {
      b0[sl] = __builtin_amdgcn_raw_buffer_load_b128(h1rs, hb0, KSLOT(2 + ks + H1_WIN) * 1024, H1_AUX);
      b1v[sl] = __builtin_amdgcn_raw_buffer_load_b128(h1rs, hb1, KSLOT(2 + ks + H1_WIN) * 1024, H1_AUX);
    }
    SCHED_FENCE();
  }
  STAMP(6);
  f32x4* red = (f32x4*)smem;
  f32x4 own0 = {}, own1 = {};
#pragma unroll
  for (int a = 0; a < 8; ++a) {
    const int u = (a ^ (2 * (wv & 1)));
    const f32x4 v0 = {acc2[a >> 1][8 * (a & 1)], acc2[a >> 1][8 * (a & 1) + 1], acc2[a >> 1][8 * (a & 1) + 2], acc2[a >> 1][8 * (a & 1) + 3]};
    const f32x4 v1 = {acc2[a >> 1][8 * (a & 1) + 4], acc2[a >> 1][8 * (a & 1) + 5], acc2[a >> 1][8 * (a & 1) + 6], acc2[a >> 1][8 * (a & 1) + 7]};
    if (u == wv) { own0 = v0; own1 = v1; }
    else {
      const int slot = u * 7 + (wv > u ? wv - 1 : wv);
      red[(slot * 2) * 64 + lane] = v0;
      red[(slot * 2 + 1) * 64 + lane] = v1;
    }
  }
  __syncthreads();
  STAMP(7);
  {
    f32x4 s0[7], s1[7];
#pragma unroll
    for (int i = 0; i < 7; ++i) { s0[i] = red[((wv * 7 + i) * 2) * 64 + lane]; s1[i] = red[((wv * 7 + i) * 2 + 1) * 64 + lane]; }
    const f32x4 o0 = ((own0 + s0[0]) + (s0[1] + s0[2])) + ((s0[3] + s0[4]) + (s0[5] + s0[6]));
    const f32x4 o1 = ((own1 + s1[0]) + (s1[1] + s1[2])) + ((s1[3] + s1[4]) + (s1[5] + s1[6]));
    const u32x4 hr = __builtin_bit_cast(u32x4, res);
    unsigned pk[4];
#pragma unroll
    for (int e = 0; e < 4; ++e) {
      const float x0 = (e < 2) ? o0[2 * e] : o1[2 * e - 4], x1 = (e < 2) ? o0[2 * e + 1] : o1[2 * e - 3];
      const unsigned ob_ = pack_bf16(x0, x1);
      const unsigned o2 = pack_bf16(bflo(ob_) + bflo(bz[e]), bfhi(ob_) + bfhi(bz[e]));
      pk[e] = pack_bf16(bflo(hr[e]) + bflo(o2), bfhi(hr[e]) + bfhi(o2));
    }
    const __amdgpu_buffer_rsrc_t ors = __builtin_amdgcn_make_buffer_rsrc((void*)out, 0, L_ * D_ * 2, 0x00020000);
    __builtin_amdgcn_raw_buffer_store_b128((u32x4){pk[0], pk[1], pk[2], pk[3]}, ors, ((tok0 + c2 * 32 + col) * D_ + d0) * 2, 0, OUT_AUX);
  }
  STAMP(8); STAMPW(14);
}

extern "C" void kernel_launch(void* const* d_in, const int* in_sizes, int n_in, void* d_out, int out_size, void* d_ws,
                              size_t ws_size, hipStream_t stream) {
  const u16* x = (const u16*)d_in[0];
  const u16* lar = (const u16*)d_in[1];
  const u16* aim = (const u16*)d_in[2];
  const u16* cr = (const u16*)d_in[3];
  const u16* ci = (const u16*)d_in[4];
  const u16* ldt = (const u16*)d_in[5];
  const u16* dsk = (const u16*)d_in[6];
  const u16* w1 = (const u16*)d_in[8];
  const u16* b1 = (const u16*)d_in[9];
  const u16* w2 = (const u16*)d_in[10];
  const u16* b2 = (const u16*)d_in[11];
  char* ws = (char*)d_ws;
  k1_kernel<<<K1_GRID, 256, 0, stream>>>(x, lar, aim, cr, ci, ldt, w1, b1, w2, ws);
  k2_kernel<<<224, 512, 0, stream>>>(dsk, b2, ws, (u16*)d_out);
}
```

```cpp
#include <hip/hip_runtime.h>
#include <hip/hip_bf16.h>
#include <stdint.h>

typedef __attribute__((ext_vector_type(8))) __bf16 bf16x8;
typedef __attribute__((ext_vector_type(4))) __bf16 bf16x4;
typedef __attribute__((ext_vector_type(2))) __bf16 bf16x2;
typedef __attribute__((ext_vector_type(8))) _Float16 f16x8;
typedef __attribute__((ext_vector_type(2))) _Float16 f16x2;
typedef __attribute__((ext_vector_type(2))) float f32x2;
typedef __attribute__((ext_vector_type(4))) float f32x4;
typedef __attribute__((ext_vector_type(16))) float f32x16;
typedef __attribute__((ext_vector_type(4))) unsigned u32x4;
typedef __attribute__((ext_vector_type(2))) unsigned u32x2;
typedef unsigned short u16;

#define DI __device__ __forceinline__
#define SCHED_FENCE() __builtin_amdgcn_sched_barrier(0)

constexpr int L_ = 2048, D_ = 448, N_ = 256, F_ = 1792;
constexpr int KR_LEN = 2304;

constexpr size_t OFF_UT = 0;
constexpr size_t OFF_XT = OFF_UT + (size_t)D_ * L_ * 2;
constexpr size_t OFF_KR = OFF_XT + (size_t)D_ * L_ * 2;
constexpr size_t OFF_KS = OFF_KR + (size_t)D_ * KR_LEN * 2;
constexpr int W1_FRAGS = 29;
constexpr size_t OFF_CNT = OFF_KS + 2048;
constexpr size_t OFF_W1 = OFF_CNT + 32768;
constexpr size_t OFF_W2 = OFF_W1 + (size_t)56 * W1_FRAGS * 1024;
constexpr int HT_LD = L_ + 128;
constexpr size_t OFF_HT = OFF_W2 + (size_t)D_ * F_ * 2;
constexpr size_t OFF_H1 = OFF_HT + (size_t)D_ * HT_LD * 2;
constexpr size_t WS_END = OFF_H1 + (size_t)L_ * F_ * 2;

DI float bf2f(u16 v) { return __uint_as_float(((unsigned)v) << 16); }
DI float bflo(unsigned p) { return __uint_as_float(p << 16); }
DI float bfhi(unsigned p) { return __uint_as_float(p & 0xffff0000u); }
DI unsigned pack_bf16(float a, float b) {
  f32x2 v = {a, b};
  return __builtin_bit_cast(unsigned, __builtin_convertvector(v, bf16x2));
}
DI unsigned pack_f16(float a, float b) {
  f32x2 v = {a, b};
  return __builtin_bit_cast(unsigned, __builtin_convertvector(v, f16x2));
}
DI float rbf(float a) { return bflo(pack_bf16(a, a)); }
template <int CTRL> DI float dpp_add(float v) {
  const int t = __builtin_amdgcn_update_dpp(0, __float_as_int(v), CTRL, 0xf, 0xf, true);
  return v + __int_as_float(t);
}
DI float wave_sum(float v) {
  v = dpp_add<0xB1>(v);
  v = dpp_add<0x4E>(v);
  v = dpp_add<0x141>(v);
  v = dpp_add<0x140>(v);
  auto r = __builtin_amdgcn_permlane16_swap(__float_as_uint(v), __float_as_uint(v), false, false);
  v = __uint_as_float(r[0]) + __uint_as_float(r[1]);
  auto r2 = __builtin_amdgcn_permlane32_swap(__float_as_uint(v), __float_as_uint(v), false, false);
  return __uint_as_float(r2[0]) + __uint_as_float(r2[1]);
}
#define LN_AUX 2
#define OUT_AUX 0
DI __amdgpu_buffer_rsrc_t ws_rsrc(char* ws) { return __builtin_amdgcn_make_buffer_rsrc((void*)ws, 0, (int)WS_END, 0x00020000); }
DI void wst128(__amdgpu_buffer_rsrc_t rs, size_t off, u32x4 v) { __builtin_amdgcn_raw_buffer_store_b128(v, rs, (unsigned)off, 0, 16); }
DI f32x2 cswz(f32x2 b) { return b; }
DI f32x2 cm_b(f32x2 t, f32x2 a, f32x2 b) {
  f32x2 o;
  asm("v_pk_fma_f32 %0, %1, %2, %3 op_sel:[1,1,0] op_sel_hi:[1,0,1] neg_lo:[0,1,0]" : "=v"(o) : "v"(a), "v"(b), "v"(t));
  return o;
}
DI f32x2 cexp_p(float ar2, float air, float p) {
  const float mag = __builtin_amdgcn_exp2f(ar2 * p);
  const float ph = __builtin_amdgcn_fractf(air * p);
  return (f32x2){mag * __builtin_amdgcn_cosf(ph), mag * __builtin_amdgcn_sinf(ph)};
}
DI unsigned pk16(f32x2 v) { return __builtin_bit_cast(unsigned, __builtin_convertvector(v, f16x2)); }
DI void pow2_scale(float s, float& scale, float& inv) {
  int ex = (s > 0.f) ? __builtin_amdgcn_frexp_expf(s) : 14;
  ex = min(max(ex, -100), 120);
  scale = ldexpf(1.f, 14 - ex);
  inv = ldexpf(1.f, ex - 14);
}
DI int perm23(int r) { return (r & ~12) | ((r & 4) << 1) | ((r & 8) >> 1); }

constexpr int K1_KGEN = 448, K1_LN = 128, K1_WT = 112;
constexpr int K1_GRID = K1_KGEN + K1_LN + K1_WT;
constexpr int KG_RS = 272;
constexpr int KG_WAVE = 32 * KG_RS;
constexpr int KG_PART = 4 * KG_WAVE;
constexpr int K1_LDS = KG_PART + 4 * 48 * 16 * 4 + 64;

struct KTab { unsigned short v[2048]; };
constexpr KTab make_ktab() {
  KTab t{};
  for (int i = 0; i < 2048; ++i) {
    const int l = 2047 - i;
    int e = 0;
    while ((l >> (8 + e)) != 0) ++e;
    int p = l;
    if (e > 0) {
      const int q = l >> e, rem = l & ((1 << e) - 1), half = 1 << (e - 1);
      p = (q + ((rem > half || (rem == half && (q & 1))) ? 1 : 0)) << e;
    }
    int e2 = 0;
    while ((p >> (8 + e2)) != 0) ++e2;
    if (e2 > 3) e2 = 3;
    const int row = 8 * e2 + ((p >> 7) << (3 - e2)) + ((p & 7) >> e2);
    const int col = (p & 127) >> 3;
    t.v[i] = (unsigned short)(row * 16 + col);
  }
  return t;
}
__device__ const KTab KTAB = make_ktab();

DI void k1_kgen(char* smem, int d, const u16* __restrict__ lar_p, const u16* __restrict__ aim_p,
                const u16* __restrict__ cr_p, const u16* __restrict__ ci_p, const u16* __restrict__ ldt_p,
                char* __restrict__ ws) {
  const int tid = threadIdx.x, lane = tid & 63;
  const int kq = __builtin_amdgcn_readfirstlane(tid >> 6);
  const int idx = d * N_ + tid;
  const float lar = bf2f(lar_p[idx]), aim = bf2f(aim_p[idx]), cr = bf2f(cr_p[idx]), ci = bf2f(ci_p[idx]);
  const float ldt = bf2f(ldt_p[d]);
  const u32x4 tab = *(const u32x4*)(&KTAB.v[tid * 8]);
  const float LOG2E = 1.4426950408889634f;
  const float dt = rbf(__builtin_amdgcn_exp2f(ldt * LOG2E));
  const float are = -rbf(__builtin_amdgcn_exp2f(lar * LOG2E));
  const float ar = are * dt, ai = aim * dt;
  const float Cr = cr * dt, Ci = ci * dt;
  float* red = (float*)(smem + K1_LDS - 64);
  const float sw = wave_sum(2.f * (fabsf(Cr) + fabsf(Ci)));
  float wscale, winv;
  pow2_scale(sw, wscale, winv);
  if (lane == 0) red[kq] = sw;
  const float ar2 = ar * LOG2E, air = ai * 0.15915494309189535f;
  char* wb = smem + kq * KG_WAVE + lane * 4;
  char* hb = wb + 16 * KG_RS;
  const int r16 = lane & 15, gq = lane >> 4;
  const char* base = smem + kq * KG_WAVE + r16 * KG_RS + gq * 16;
  f32x4 acc[3] = {};
  f16x8 bfr[4];
#define CM_A(t, a, b) const f32x2 t = (f32x2){(a).x, (a).x} * (b)
#define CM_B(t, a, bs) cm_b(t, a, bs)
  f32x2 Lo[16], g[8], R[8], z[9];
  const f32x2 E1 = cexp_p(ar2, air, 1.f);
  Lo[1] = cexp_p(ar2, -air, 8.f);
  R[1] = cexp_p(ar2, air, 128.f);
  g[0] = (f32x2){2.f * wscale * Cr, 2.f * wscale * Ci};
  const f32x2 g0s = cswz(g[0]), R1s = cswz(R[1]);
  f32x2 E2, E4, R8, R16;
  {
    const f32x2 E1s = cswz(E1), L1s = cswz(Lo[1]);
    CM_A(t0, E1, E1); CM_A(t1, Lo[1], Lo[1]); CM_A(t2, R[1], R[1]); CM_A(t3, E1, g[0]);
    SCHED_FENCE();
    E2 = CM_B(t0, E1, E1s); Lo[2] = CM_B(t1, Lo[1], L1s); R[2] = CM_B(t2, R[1], R1s); g[1] = CM_B(t3, E1, g0s);
    SCHED_FENCE();
  }
  const f32x2 E2s = cswz(E2), L2s = cswz(Lo[2]), R2s = cswz(R[2]);
  {
    CM_A(t0, E2, E2); CM_A(t1, Lo[2], Lo[2]); CM_A(t2, R[2], R[2]); CM_A(t3, g[0], E2); CM_A(t4, g[1], E2);
    CM_A(t5, Lo[1], Lo[2]); CM_A(t6, R[1], R[2]);
    SCHED_FENCE();
    E4 = CM_B(t0, E2, E2s); Lo[4] = CM_B(t1, Lo[2], L2s); R[4] = CM_B(t2, R[2], R2s); g[2] = CM_B(t3, g[0], E2s);
    g[3] = CM_B(t4, g[1], E2s); Lo[3] = CM_B(t5, Lo[1], L2s); R[3] = CM_B(t6, R[1], R2s);
    SCHED_FENCE();
  }
  const f32x2 E4s = cswz(E4), L4s = cswz(Lo[4]), R4s = cswz(R[4]);
  {
    f32x2 t[12];
#pragma unroll
    for (int k = 0; k < 4; ++k) {
      t[k] = (f32x2){g[k].x, g[k].x} * E4;
      t[4 + k] = (f32x2){Lo[k + 1].x, Lo[k + 1].x} * Lo[4];
      t[8 + k] = (f32x2){R[k + 1].x, R[k + 1].x} * R[4];
    }
    SCHED_FENCE();
#pragma unroll
    for (int k = 0; k < 4; ++k) {
      g[4 + k] = CM_B(t[k], g[k], E4s);
      Lo[5 + k] = CM_B(t[4 + k], Lo[k + 1], L4s);
      if (k < 3) R[5 + k] = CM_B(t[8 + k], R[k + 1], R4s); else R8 = CM_B(t[8 + k], R[4], R4s);
    }
    SCHED_FENCE();
  }
  {
    const f32x2 L8s = cswz(Lo[8]), R8s = cswz(R8), R3s = cswz(R[3]), g4s = cswz(g[4]);
    f32x2 t[33];
#pragma unroll
    for (int k = 1; k < 8; ++k) t[24 + k] = (f32x2){Lo[k].x, Lo[k].x} * Lo[8];
    t[24] = (f32x2){g[0].x, g[0].x} * R8;
    t[32] = (f32x2){R8.x, R8.x} * R8;
#pragma unroll
    for (int c = 0; c < 8; ++c) t[c] = (f32x2){g[c].x, g[c].x} * R[1];
#pragma unroll
    for (int c = 0; c < 4; ++c) { t[8 + c] = (f32x2){g[2 * c].x, g[2 * c].x} * R[2]; t[12 + c] = (f32x2){g[2 * c].x, g[2 * c].x} * R[3]; }
#pragma unroll
    for (int h = 0; h < 4; ++h) { t[16 + 2 * h] = (f32x2){R[4 + h].x, R[4 + h].x} * g[0]; t[17 + 2 * h] = (f32x2){R[4 + h].x, R[4 + h].x} * g[4]; }
    SCHED_FENCE();
    z[0] = CM_B(t[24], g[0], R8s);
    R16 = CM_B(t[32], R8, R8s);
#pragma unroll
    for (int k = 1; k < 8; ++k) Lo[8 + k] = CM_B(t[24 + k], Lo[k], L8s);
#pragma unroll
    for (int c = 0; c < 8; ++c) t[c] = CM_B(t[c], g[c], R1s);
#pragma unroll
    for (int c = 0; c < 4; ++c) { t[8 + c] = CM_B(t[8 + c], g[2 * c], R2s); t[12 + c] = CM_B(t[12 + c], g[2 * c], R3s); }
#pragma unroll
    for (int h = 0; h < 4; ++h) { t[16 + 2 * h] = CM_B(t[16 + 2 * h], R[4 + h], g0s); t[17 + 2 * h] = CM_B(t[17 + 2 * h], R[4 + h], g4s); }
    SCHED_FENCE();
    *(unsigned*)(wb) = 0x00003c00u;
#pragma unroll
    for (int k = 1; k < 16; ++k) *(unsigned*)(wb + k * KG_RS) = pk16(Lo[k]);
#pragma unroll
    for (int c = 0; c < 8; ++c) *(unsigned*)(hb + c * KG_RS) = pk16(g[c]);
#pragma unroll
    for (int i = 0; i < 8; ++i) *(unsigned*)(hb + (8 + i) * KG_RS) = pk16(t[i]);
#pragma unroll
    for (int ks = 0; ks < 4; ++ks) {
      bfr[ks] = *(const f16x8*)(base + ks * 64);
      acc[0] = __builtin_amdgcn_mfma_f32_16x16x32_f16(*(const f16x8*)(base + 16 * KG_RS + ks * 64), bfr[ks], acc[0], 0, 0, 0);
    }
#pragma unroll
    for (int i = 0; i < 16; ++i) *(unsigned*)(hb + i * KG_RS) = pk16(t[8 + i]);
#pragma unroll
    for (int ks = 0; ks < 4; ++ks)
      acc[1] = __builtin_amdgcn_mfma_f32_16x16x32_f16(*(const f16x8*)(base + 16 * KG_RS + ks * 64), bfr[ks], acc[1], 0, 0, 0);
    SCHED_FENCE();
  }
  {
    const f32x2 z0s = cswz(z[0]);
    f32x2 t[9];
#pragma unroll
    for (int k = 1; k < 8; ++k) t[k] = (f32x2){R[k].x, R[k].x} * z[0];
    t[8] = (f32x2){R16.x, R16.x} * g[0];
    SCHED_FENCE();
#pragma unroll
    for (int k = 1; k < 8; ++k) z[k] = CM_B(t[k], R[k], z0s);
    z[8] = CM_B(t[8], R16, g0s);
    SCHED_FENCE();
#pragma unroll
    for (int h = 0; h < 9; ++h) *(unsigned*)(hb + h * KG_RS) = pk16(z[h]);
#pragma unroll
    for (int ks = 0; ks < 4; ++ks)
      acc[2] = __builtin_amdgcn_mfma_f32_16x16x32_f16(*(const f16x8*)(base + 16 * KG_RS + ks * 64), bfr[ks], acc[2], 0, 0, 0);
  }
  {
    float* part = (float*)(smem + KG_PART) + kq;
#pragma unroll
    for (int m = 0; m < 3; ++m)
#pragma unroll
      for (int j = 0; j < 4; ++j) part[((16 * m + 4 * gq + j) * 16 + r16) * 4] = acc[m][j] * winv;
  }
  __syncthreads();
  {
    float scale, inv_scale;
    pow2_scale((red[0] + red[1]) + (red[2] + red[3]), scale, inv_scale);
    const f32x4* part = (const f32x4*)(smem + KG_PART);
    unsigned pk[4];
#pragma unroll
    for (int j = 0; j < 4; ++j) {
      const f32x4 p0 = part[tab[j] & 0xffffu], p1 = part[tab[j] >> 16];
      pk[j] = pack_f16(((p0[0] + p0[1]) + (p0[2] + p0[3])) * scale, ((p1[0] + p1[1]) + (p1[2] + p1[3])) * scale);
    }
    const __amdgpu_buffer_rsrc_t rs = ws_rsrc(ws);
    const size_t kr = OFF_KR + (size_t)d * KR_LEN * 2;
    wst128(rs, kr + tid * 16, (u32x4){pk[0], pk[1], pk[2], pk[3]});
    if (tid < 32) wst128(rs, kr + 4096 + tid * 16, (u32x4){0u, 0u, 0u, 0u});
    if (tid == 0) ((float*)(ws + OFF_KS))[d] = inv_scale;
  }
}

DI float reduce4(const float (&a)[4]) {
  float v[2];
#pragma unroll
  for (int i = 0; i < 2; ++i) {
    auto r = __builtin_amdgcn_permlane32_swap(__float_as_uint(a[i]), __float_as_uint(a[i + 2]), false, false);
    v[i] = __uint_as_float(r[0]) + __uint_as_float(r[1]);
  }
  auto r = __builtin_amdgcn_permlane16_swap(__float_as_uint(v[0]), __float_as_uint(v[1]), false, false);
  float x = __uint_as_float(r[0]) + __uint_as_float(r[1]);
  x = dpp_add<0xB1>(x);
  x = dpp_add<0x4E>(x);
  x = dpp_add<0x141>(x);
  x = dpp_add<0x140>(x);
  return x;
}
DI float bcast_row(float w, int t) { return __uint_as_float(__builtin_amdgcn_readlane(__float_as_uint(w), 16 * t)); }

constexpr int LN_PL = 448 * 32 + 56 * 16;
DI void k1_ln(char* smem, int blk, const u16* __restrict__ x, char* __restrict__ ws) {
  const int tid = threadIdx.x, lane = tid & 63, wv = tid >> 6;
  const int tok0 = blk * 16 + wv * 4;
  const bool act = lane < 56;
  u32x4 xr[4];
#pragma unroll
  for (int t = 0; t < 4; ++t) {
    xr[t] = (u32x4){0u, 0u, 0u, 0u};
    if (act) xr[t] = *(const u32x4*)(x + (size_t)(tok0 + t) * D_ + lane * 8);
  }
  f32x2 xf[4][4];
  float part[4];
#pragma unroll
  for (int t = 0; t < 4; ++t) {
#pragma unroll
    for (int j = 0; j < 4; ++j) xf[t][j] = (f32x2){bflo(xr[t][j]), bfhi(xr[t][j])};
    const f32x2 s2 = (xf[t][0] + xf[t][1]) + (xf[t][2] + xf[t][3]);
    part[t] = s2.x + s2.y;
  }
  const float wm = reduce4(part) * (1.f / D_);
#pragma unroll
  for (int t = 0; t < 4; ++t) {
    const float m = bcast_row(wm, t);
    f32x2 q2 = {0.f, 0.f};
#pragma unroll
    for (int j = 0; j < 4; ++j) { const f32x2 dd = xf[t][j] - (f32x2){m, m}; q2 = __builtin_elementwise_fma(dd, dd, q2); }
    part[t] = act ? q2.x + q2.y : 0.f;
  }
  const float wq = reduce4(part);
  const float sd = rbf(sqrtf(rbf(rbf(wq * (1.f / D_)) + 1e-5f)));
  const float wr = 1.f / sd;
  const float wb = rbf(wm);
  f32x2 un[4][4];
#pragma unroll
  for (int t = 0; t < 4; ++t) {
    const float mb = bcast_row(wb, t), ri = bcast_row(wr, t);
#pragma unroll
    for (int j = 0; j < 4; ++j) {
      const f32x2 dd = xf[t][j] - (f32x2){mb, mb};
      const unsigned pb = pack_bf16(dd.x, dd.y);
      un[t][j] = (f32x2){bflo(pb), bfhi(pb)} * (f32x2){ri, ri};
    }
  }
  if (act) {
    char* ub = smem + lane * (8 * 32 + 16) + wv * 8;
#pragma unroll
    for (int j = 0; j < 8; ++j) {
      u32x2 pu, px;
#pragma unroll
      for (int t = 0; t < 2; ++t) {
        pu[t] = pack_bf16(un[2 * t][j >> 1][j & 1], un[2 * t + 1][j >> 1][j & 1]);
        const unsigned a = xr[2 * t][j >> 1], b = xr[2 * t + 1][j >> 1];
        px[t] = (j & 1) ? __builtin_amdgcn_perm(b, a, 0x07060302u) : __builtin_amdgcn_perm(b, a, 0x05040100u);
      }
      *(u32x2*)(ub + j * 32) = pu;
      *(u32x2*)(ub + LN_PL + j * 32) = px;
    }
  }
  __syncthreads();
  const __amdgpu_buffer_rsrc_t wrs = ws_rsrc(ws);
#pragma unroll
  for (int i = 0; i < 4; ++i) {
    const int ch = i * 128 + (tid >> 1), v = tid & 1;
    if (ch < D_) {
      const int o = ch * 32 + (ch >> 3) * 16 + v * 16;
      const u32x4 a = *(const u32x4*)(smem + o);
      const u32x4 b = *(const u32x4*)(smem + LN_PL + o);
      __builtin_amdgcn_raw_buffer_store_b128(a, wrs, (unsigned)(OFF_UT + ((size_t)ch * L_ + blk * 16 + v * 8) * 2), 0, LN_AUX);
      __builtin_amdgcn_raw_buffer_store_b128(b, wrs, (unsigned)(OFF_XT + ((size_t)ch * L_ + blk * 16 + v * 8) * 2), 0, LN_AUX);
    }
  }
}

typedef __attribute__((address_space(3))) bf16x4* lds_bf16x4_p;
DI void k1_wtile(char* smem, const u16* __restrict__ src, int ldn, size_t dsto, const u16* __restrict__ b1s, char* ws) {
  const int tid = threadIdx.x, lane = tid & 63, wv = tid >> 6;
  const __amdgpu_buffer_rsrc_t rs = ws_rsrc(ws);
  u32x4 v[7];
#pragma unroll
  for (int i = 0; i < 7; ++i) {
    const int c = i * 256 + tid, k = c >> 2, q = c & 3;
    v[i] = *(const u32x4*)(src + (size_t)k * ldn + q * 8);
  }
  unsigned b1v = 0;
  if (b1s) b1v = b1s[perm23(tid & 31)];
  SCHED_FENCE();
  {
    const int q = tid & 3;
    char* dst = smem + (tid >> 2) * 64 + 32 * (q >> 1) + 8 * (q & 1);
#pragma unroll
    for (int i = 0; i < 7; ++i) {
      *(u32x2*)(dst + i * 4096) = (u32x2){v[i][0], v[i][1]};
      *(u32x2*)(dst + i * 4096 + 16) = (u32x2){v[i][2], v[i][3]};
    }
  }
  __syncthreads();
  {
    const int hh = lane >> 5, blk = (lane >> 4) & 1, q = (lane & 15) >> 2, p = lane & 3;
    const char* rd = smem + (8 * hh + q) * 64 + 32 * blk + 8 * p + wv * 1024;
#pragma unroll
    for (int i = 0; i < 7; ++i) {
      const bf16x4 lo = __builtin_amdgcn_ds_read_tr16_b64_v4bf16((lds_bf16x4_p)(rd + i * 4096));
      const bf16x4 hi = __builtin_amdgcn_ds_read_tr16_b64_v4bf16((lds_bf16x4_p)(rd + i * 4096 + 256));
      const bf16x8 f = __builtin_shufflevector(lo, hi, 0, 1, 2, 3, 4, 5, 6, 7);
      wst128(rs, dsto + (size_t)((i * 4 + wv) * 64 + lane) * 16, __builtin_bit_cast(u32x4, f));
    }
  }
  if (b1s) {
    const int cp = tid & 15, kg = tid >> 4;
    f32x2 sacc = {0.f, 0.f};
#pragma unroll
    for (int k = 0; k < 28; ++k) {
      const unsigned w = *(const unsigned*)(smem + (kg * 28 + k) * 64 + cp * 4);
      sacc += (f32x2){bflo(w), bfhi(w)};
    }
    f32x2* red = (f32x2*)(smem + 448 * 64);
    red[tid] = sacc;
    __syncthreads();
    if (tid < 64) {
      const float* redf = (const float*)red;
      float a = 0.f;
#pragma unroll
      for (int g = 0; g < 16; ++g) a += redf[g * 32 + (tid & 31)];
      const float hi = rbf(a);
      const unsigned w0 = pack_bf16(hi, a - hi);
      wst128(rs, dsto + (size_t)(28 * 64 + tid) * 16, (u32x4){tid < 32 ? w0 : 0u, tid < 32 ? b1v : 0u, 0u, 0u});
    }
  }
}

__global__ __launch_bounds__(256, 3) void k1_kernel(const u16* __restrict__ x, const u16* __restrict__ lar,
                                                    const u16* __restrict__ aim, const u16* __restrict__ cr,
                                                    const u16* __restrict__ ci, const u16* __restrict__ ldt,
                                                    const u16* __restrict__ w1, const u16* __restrict__ b1,
                                                    const u16* __restrict__ w2, char* __restrict__ ws) {
  __shared__ __attribute__((aligned(16))) char smem[K1_LDS];
  int b = blockIdx.x;
  if (b < K1_LN) {
    if (b == 0 && threadIdx.x < 65) ((unsigned*)(ws + OFF_CNT))[threadIdx.x * 64] = 0u;
    k1_ln(smem, b, x, ws);
    return;
  }
  b -= K1_LN;
  if (b < K1_KGEN) { k1_kgen(smem, b, lar, aim, cr, ci, ldt, ws); return; }
  b -= K1_KGEN;
  if (b < 56) {
    k1_wtile(smem, w1 + b * 32, F_, OFF_W1 + (size_t)b * W1_FRAGS * 1024, b1 + b * 32, ws);
  } else {
    const int dt = (b - 56) >> 2, kc = (b - 56) & 3;
    k1_wtile(smem, w2 + (size_t)kc * 448 * D_ + dt * 32, D_, OFF_W2 + ((size_t)dt * 112 + kc * 28) * 1024, nullptr, ws);
  }
}

constexpr int CV_CS = 4160;
constexpr int CV_KOFF = 8 * CV_CS;
constexpr int CV_CH = CV_KOFF + KR_LEN * 2;
constexpr int K3_TILE = 448 * 128;
constexpr int K3_RED = 8 * 7 * 2048;
constexpr int K3_STAT = K3_RED;
constexpr int K3_LDS = 2 * CV_CH + 2 * 24576;
static_assert(K3_STAT + 4 * 64 * 8 <= K3_LDS && (CV_CH & 15) == 0, "lds");
typedef __attribute__((address_space(1))) unsigned gu32;

DI bf16x8 k3_bfrag(const char* smem, unsigned lane_off0, unsigned lane_off1, int off) {
  const bf16x4 lo = __builtin_amdgcn_ds_read_tr16_b64_v4bf16((lds_bf16x4_p)(smem + lane_off0 + off));
  const bf16x4 hi = __builtin_amdgcn_ds_read_tr16_b64_v4bf16((lds_bf16x4_p)(smem + lane_off1 + off));
  return __builtin_shufflevector(lo, hi, 0, 1, 2, 3, 4, 5, 6, 7);
}
#define W1_EARLY 5
#define H1_AUX 16
#define POLL_SLEEP 1
#define H1_WIN 6
template <int N> DI void wait_vm() { asm volatile("s_waitcnt vmcnt(%0)" ::"i"(N) : "memory"); }

__global__ __launch_bounds__(512, 2) void k2_kernel(const u16* __restrict__ dskip, const u16* __restrict__ b2,
                                                    char* __restrict__ ws, u16* __restrict__ out) {
  __shared__ __attribute__((aligned(16))) char smem[K3_LDS];
  const int tid = threadIdx.x, lane = tid & 63;
  const int wv = __builtin_amdgcn_readfirstlane(tid >> 6);
  const int xcd = blockIdx.x & 7, idx = blockIdx.x >> 3;
  const int grp = 4 * xcd + idx / 7, j = idx % 7;
  const int tok0 = grp * 64, ft = j * 8 + wv;
  const int sh = (wv >> 1) & 1;
  const int s2 = wv >> 2;
  const __amdgpu_buffer_rsrc_t wrs = ws_rsrc(ws);
  const bf16x8* w1f = (const bf16x8*)(ws + OFF_W1) + (size_t)ft * W1_FRAGS * 64 + lane;
  const bf16x8* w1a = w1f + sh * 128;
  const bf16x8* w1b = w1f + (1 - sh) * 128;
  bf16x8 A[28];
#define ISSUEA(c) do { A[4 * (c)] = w1a[(4 * (c)) * 64]; A[4 * (c) + 1] = w1a[(4 * (c) + 1) * 64]; \
    A[4 * (c) + 2] = w1b[(4 * (c)) * 64]; A[4 * (c) + 3] = w1b[(4 * (c) + 1) * 64]; } while (0)
#define LDA(n) ((((n) & 2) == 0) ? w1a[(4 * ((n) >> 2) + ((n) & 1)) * 64] : w1b[(4 * ((n) >> 2) + ((n) & 1)) * 64])
  bf16x8 Aext;
  {
    const int d = 2 * blockIdx.x + (wv >> 2), cw = wv & 3, m0 = tid & 255;
    char* cs = smem + (wv >> 2) * CV_CH;
    const u16* uT = (const u16*)(ws + OFF_UT) + (size_t)d * L_;
    const u16* xT = (const u16*)(ws + OFF_XT) + (size_t)d * L_;
    const char* kr = ws + OFF_KR + (size_t)d * KR_LEN * 2;
    u32x4 c0 = {0u, 0u, 0u, 0u};
    if (m0 > 0) c0 = *(const u32x4*)(uT + (m0 - 1) * 8);
    const u32x4 c1 = *(const u32x4*)(uT + m0 * 8);
    const u32x4 k0 = *(const u32x4*)(kr + m0 * 16);
    u32x4 k1 = {0u, 0u, 0u, 0u};
    if (m0 < 32) k1 = *(const u32x4*)(kr + 4096 + m0 * 16);
    const int c16 = lane & 15, g4 = lane >> 4;
    u32x2 ue[2], xe[2];
#pragma unroll
    for (int i = 0; i < 2; ++i) {
      const int pp = 256 * (2 * cw + i) + 16 * c16 + 4 * g4;
      ue[i] = *(const u32x2*)(uT + pp);
      xe[i] = *(const u32x2*)(xT + pp);
    }
    const float inv_scale = ((const float*)(ws + OFF_KS))[d];
    const float dsk = bf2f(dskip[d]);
    SCHED_FENCE();
    *(u32x4*)(cs + CV_KOFF + m0 * 16) = k0;
    if (m0 < 32) *(u32x4*)(cs + CV_KOFF + 4096 + m0 * 16) = k1;
    if (m0 < 8) *(u32x4*)(cs + m0 * CV_CS) = (u32x4){0u, 0u, 0u, 0u};
    {
      unsigned pq[8];
#pragma unroll
      for (int e = 0; e < 4; ++e) {
        pq[e] = pack_f16(bflo(c0[e]), bfhi(c0[e]));
        pq[4 + e] = pack_f16(bflo(c1[e]), bfhi(c1[e]));
      }
      unsigned qq[7];
#pragma unroll
      for (int i = 0; i < 7; ++i) qq[i] = __builtin_amdgcn_alignbit(pq[i + 1], pq[i], 16);
      char* dst = cs + (m0 + 1) * 16;
#pragma unroll
      for (int rho = 0; rho < 8; ++rho) {
        const int k = (rho + 1) >> 1;
        const u32x4 w = (rho & 1) ? (u32x4){pq[k], pq[k + 1], pq[k + 2], pq[k + 3]} : (u32x4){qq[k], qq[k + 1], qq[k + 2], qq[k + 3]};
        *(u32x4*)(dst + rho * CV_CS) = w;
      }
    }
    __syncthreads();
    f32x4 cacc[8] = {};
    const char* abase = cs + (c16 & 7) * CV_CS + ((c16 & 8) + 8 * g4) * 2;
    const char* bbase = cs + CV_KOFF + (2032 - 16 * c16 + 8 * g4) * 2;
#pragma unroll
    for (int si = 0; si < 2; ++si) {
      const int sg = 2 * cw + si;
      f16x8 CA[8], CB[8];
#pragma unroll
      for (int i = 0; i < 8; ++i) {
        CA[i] = *(const f16x8*)(abase + sg * 64 + i * 512);
        CB[i] = *(const f16x8*)(bbase + sg * 64 - i * 512);
      }
#pragma unroll
      for (int t = 0; t < 8; ++t) {
#pragma unroll
        for (int ia = 0; ia <= t; ++ia)
          cacc[t] = __builtin_amdgcn_mfma_f32_16x16x32_f16(CA[ia], CB[t - ia], cacc[t], 0, 0, 0);
        if (t >= 3) {
          const int n0 = (si * 5 + (t - 3)) * 2;
          if (n0 < 4 * W1_EARLY) { A[n0] = LDA(n0); A[n0 + 1] = LDA(n0 + 1); }
          SCHED_FENCE();
        }
      }
      SCHED_FENCE();
    }
    f32x4* red = (f32x4*)(smem + 2 * CV_CH + (wv >> 2) * 24576);
    f32x4 own[2] = {};
#pragma unroll
    for (int t = 0; t < 8; ++t) {
      if ((t >> 1) == cw) own[t & 1] = cacc[t];
      else red[(t * 3 + (cw > (t >> 1) ? cw - 1 : cw)) * 64 + lane] = cacc[t];
    }
    __syncthreads();
#pragma unroll
    for (int i = 0; i < 2; ++i) {
      const int t = 2 * cw + i;
      const f32x4 sum = (own[i] + red[(t * 3) * 64 + lane]) + (red[(t * 3 + 1) * 64 + lane] + red[(t * 3 + 2) * 64 + lane]);
      float h[4];
#pragma unroll
      for (int e = 0; e < 4; ++e) {
        const float uu = (e & 1) ? bfhi(ue[i][e >> 1]) : bflo(ue[i][e >> 1]);
        const float xx = (e & 1) ? bfhi(xe[i][e >> 1]) : bflo(xe[i][e >> 1]);
        const float y = rbf(sum[e] * inv_scale);
        const float tt = rbf(y + rbf(uu * dsk));
        h[e] = xx + tt;
      }
      __builtin_amdgcn_raw_buffer_store_b64((u32x2){pack_bf16(h[0], h[1]), pack_bf16(h[2], h[3])}, wrs,
                                            (unsigned)(OFF_HT + ((size_t)d * HT_LD + 256 * t + 16 * c16 + 4 * g4) * 2), 0, 16);
    }
  }
  wait_vm<0>();
  __builtin_amdgcn_s_barrier();
  asm volatile("" ::: "memory");
  if (wv == 0) {
    gu32* gc = (gu32*)(ws + OFF_CNT) + 32 * 64;
    unsigned last = 0;
    if (lane == 0) {
      if (__hip_atomic_fetch_add(gc + (blockIdx.x & 15) * 64, 1u, __ATOMIC_RELAXED, __HIP_MEMORY_SCOPE_AGENT) == 13u)
        last = __hip_atomic_fetch_add(gc + 16 * 64, 1u, __ATOMIC_RELAXED, __HIP_MEMORY_SCOPE_AGENT) == 15u;
    }
    if (__builtin_amdgcn_readfirstlane(last)) {
      if (lane < 16) __hip_atomic_store(gc + (17 + lane) * 64, 1u, __ATOMIC_RELAXED, __HIP_MEMORY_SCOPE_AGENT);
    } else if (lane == 0) {
      for (unsigned spins = 0; spins < (1u << 20); ++spins) {
        if (__hip_atomic_load(gc + (17 + (blockIdx.x & 15)) * 64, __ATOMIC_RELAXED, __HIP_MEMORY_SCOPE_AGENT) != 0u) break;
        __builtin_amdgcn_s_sleep(POLL_SLEEP);
      }
    }
    __builtin_amdgcn_fence(__ATOMIC_ACQUIRE, "wavefront");
  }
  SCHED_FENCE();
#pragma unroll
  for (int c = W1_EARLY; c < 7; ++c) ISSUEA(c);
  Aext = w1f[28 * 64];
  SCHED_FENCE();
  __builtin_amdgcn_s_barrier();
  __builtin_amdgcn_fence(__ATOMIC_ACQUIRE, "wavefront");
  asm volatile("" ::: "memory");
  const unsigned hsrc = (unsigned)(OFF_HT + ((size_t)(tid >> 3) * HT_LD + tok0 + (tid & 7) * 8) * 2);
  const __amdgpu_buffer_rsrc_t w2rs = __builtin_amdgcn_make_buffer_rsrc((void*)(ws + OFF_W2), 0, D_ * F_ * 2, 0x00020000);
  const unsigned w2v = ((2 * j) * 112 * 64 + lane) * 16;
#define KSLOT(sl) ((sl) < 2 ? 16 * j + 2 * wv + (sl) : (12 * wv + (sl) - 2) + ((12 * wv + (sl) - 2) >= 16 * j ? 16 : 0))
#define LDW2(sl, dt) __builtin_bit_cast(bf16x8, __builtin_amdgcn_raw_buffer_load_b128(w2rs, w2v + (dt) * 112 * 1024, KSLOT(sl) * 1024, 0))
  u32x4 st[7];
  bf16x8 a0[14], a1[14];
#define ISSUE2(c) do { a0[2 * (c)] = LDW2(2 * (c), 0); a1[2 * (c)] = LDW2(2 * (c), 1); \
    a0[2 * (c) + 1] = LDW2(2 * (c) + 1, 0); a1[2 * (c) + 1] = LDW2(2 * (c) + 1, 1); } while (0)
#pragma unroll
  for (int c = 0; c < 7; ++c) st[c] = __builtin_amdgcn_raw_buffer_load_b128(wrs, hsrc, 64 * c * HT_LD * 2, 16);
  SCHED_FENCE();
  const int hh = lane >> 5, col = lane & 31;
  const unsigned q = (lane & 15) >> 2, p = lane & 3, blk = (lane >> 4) & 1;
  char* lw = smem + 128 * (tid >> 3) + 16 * ((tid & 7) ^ (((tid >> 4) & 1) << 2));
  unsigned oa[2][2], ob[2][2];
  {
    const unsigned r0 = 8 * hh + q, r1 = 8 * hh + 4 + q, sw = ((q >> 1) & 1) << 2;
#pragma unroll
    for (int it = 0; it < 2; ++it) {
      const unsigned c = (wv & 1) ^ it;
      const unsigned chk = (4 * c + 2 * blk + (p >> 1)) ^ sw;
      const unsigned x0 = 128 * r0 + 16 * chk + 8 * (p & 1), x1 = 128 * r1 + 16 * chk + 8 * (p & 1);
      oa[it][0] = x0 + sh * 4096; oa[it][1] = x1 + sh * 4096;
      ob[it][0] = x0 + (1 - sh) * 4096; ob[it][1] = x1 + (1 - sh) * 4096;
    }
  }
  f32x16 acc[2] = {};
  float ssum = 0.f, ssq = 0.f;
  const bf16x2 ones2 = {(__bf16)1.0f, (__bf16)1.0f};
#define WRITE1(c) do { *(u32x4*)(lw + (c) * 8192) = st[c]; } while (0)
#define RDB(n, it) ((((n) & 2) == 0) ? k3_bfrag(smem, oa[it][0], oa[it][1], ((n) >> 2) * 8192 + ((n) & 1) * 2048) \
                                      : k3_bfrag(smem, ob[it][0], ob[it][1], ((n) >> 2) * 8192 + ((n) & 1) * 2048))
  bf16x8 Bf[3][2];
#pragma unroll
  for (int c = 0; c < 7; ++c) WRITE1(c);
  __syncthreads();
  Bf[0][0] = RDB(0, 0); Bf[0][1] = RDB(0, 1); Bf[1][0] = RDB(1, 0); Bf[1][1] = RDB(1, 1);
#pragma unroll
  for (int n = 0; n < 28; ++n) {
    const int c = n >> 2, i = n & 3;
    acc[0] = __builtin_amdgcn_mfma_f32_32x32x16_bf16(A[n], Bf[n % 3][0], acc[0], 0, 0, 0);
    if (n + 2 < 28) Bf[(n + 2) % 3][0] = RDB(n + 2, 0);
    if (i < 2) {
      const u32x4 B = __builtin_bit_cast(u32x4, Bf[n % 3][0]);
      const bf16x2 pr = __builtin_bit_cast(bf16x2, s2 ? B[2] : B[0]);
      ssum = __builtin_amdgcn_fdot2_f32_bf16(pr, ones2, ssum, false);
      ssq = __builtin_amdgcn_fdot2_f32_bf16(pr, pr, ssq, false);
    }
    SCHED_FENCE();
    acc[1] = __builtin_amdgcn_mfma_f32_32x32x16_bf16(A[n], Bf[n % 3][1], acc[1], 0, 0, 0);
    if (n + 2 < 28) Bf[(n + 2) % 3][1] = RDB(n + 2, 1);
    if (i < 2) {
      const u32x4 B = __builtin_bit_cast(u32x4, Bf[n % 3][0]);
      const bf16x2 pr = __builtin_bit_cast(bf16x2, s2 ? B[3] : B[1]);
      ssum = __builtin_amdgcn_fdot2_f32_bf16(pr, ones2, ssum, false);
      ssq = __builtin_amdgcn_fdot2_f32_bf16(pr, pr, ssq, false);
    }
    if (i == 3) ISSUE2(c);
    SCHED_FENCE();
  }
  float2* stats = (float2*)(smem + K3_STAT);
  {
    auto r1 = __builtin_amdgcn_permlane32_swap(__float_as_uint(ssum), __float_as_uint(ssum), false, false);
    ssum = __uint_as_float(r1[0]) + __uint_as_float(r1[1]);
    auto r2 = __builtin_amdgcn_permlane32_swap(__float_as_uint(ssq), __float_as_uint(ssq), false, false);
    ssq = __uint_as_float(r2[0]) + __uint_as_float(r2[1]);
    if (hh == 0) stats[(wv >> 1) * 64 + (wv & 1) * 32 + col] = make_float2(ssum, ssq);
  }
  const int t2 = wv >> 1, hf2 = wv & 1;
  const int c2 = t2 & 1;
  bf16x8 res;
  {
    const unsigned r0 = 8 * hh + q, r1 = 8 * hh + 4 + q, sw = ((q >> 1) & 1) << 2;
    const unsigned chk = (4 * c2 + 2 * blk + (p >> 1)) ^ sw;
    res = k3_bfrag(smem, 128 * r0 + 16 * chk + 8 * (p & 1), 128 * r1 + 16 * chk + 8 * (p & 1), (j * 4 + (t2 >> 1) * 2 + hf2) * 2048);
  }
  __syncthreads();
  float sdv[2], c1v[2], c2v[2];
#pragma unroll
  for (int it = 0; it < 2; ++it) {
    const unsigned c = (wv & 1) ^ it;
    const float2 s0 = stats[c * 32 + col], s1 = stats[64 + c * 32 + col], s2_ = stats[128 + c * 32 + col], s3 = stats[192 + c * 32 + col];
    const float mean = ((s0.x + s1.x) + (s2_.x + s3.x)) * (1.f / D_);
    const float var = fmaxf(__builtin_fmaf(-mean, mean, ((s0.y + s1.y) + (s2_.y + s3.y)) * (1.f / D_)), 0.f);
    const float sd = rbf(sqrtf(rbf(rbf(var) + 1e-5f)));
    const float rs = __builtin_amdgcn_rcpf(sd);
    sdv[it] = sd;
    c1v[it] = rs * -2.3022082f;
    c2v[it] = rs * rs * rs * -0.10294324f;
    const unsigned w0 = pack_bf16(-mean, -mean), w1 = pack_bf16(sd, 0.f);
    const u32x4 bw = {hh ? 0u : w0, hh ? 0u : w1, 0u, 0u};
    acc[it] = __builtin_amdgcn_mfma_f32_32x32x16_bf16(Aext, __builtin_bit_cast(bf16x8, bw), acc[it], 0, 0, 0);
  }
  const __amdgpu_buffer_rsrc_t h1rs = __builtin_amdgcn_make_buffer_rsrc((void*)(ws + OFF_H1), 0, L_ * F_ * 2, 0x00020000);
  u32x4 own[2][2];
#pragma unroll
  for (int it = 0; it < 2; ++it) {
    const unsigned c = (wv & 1) ^ it;
    const unsigned fb = (((grp * 2 + c) * 112 + 2 * ft) * 64 + lane) * 16;
    const f32x2 c1p = {c1v[it], c1v[it]}, c2p = {c2v[it], c2v[it]}, sdp = {sdv[it], sdv[it]};
#pragma unroll
    for (int hf = 0; hf < 2; ++hf) {
      f32x2 a[4], z[4];
#pragma unroll
      for (int e = 0; e < 4; ++e) a[e] = (f32x2){acc[it][8 * hf + 2 * e], acc[it][8 * hf + 2 * e + 1]};
#pragma unroll
      for (int e = 0; e < 4; ++e) z[e] = a[e] * a[e];
#pragma unroll
      for (int e = 0; e < 4; ++e) z[e] = __builtin_elementwise_fma(z[e], c2p, c1p);
#pragma unroll
      for (int e = 0; e < 4; ++e) z[e] = a[e] * z[e];
#pragma unroll
      for (int e = 0; e < 4; ++e) z[e] = (f32x2){__builtin_amdgcn_exp2f(z[e].x), __builtin_amdgcn_exp2f(z[e].y)};
#pragma unroll
      for (int e = 0; e < 4; ++e) z[e] = __builtin_elementwise_fma(z[e], sdp, sdp);
#pragma unroll
      for (int e = 0; e < 4; ++e) z[e] = (f32x2){__builtin_amdgcn_rcpf(z[e].x), __builtin_amdgcn_rcpf(z[e].y)};
#pragma unroll
      for (int e = 0; e < 4; ++e) z[e] = a[e] * z[e];
      const u32x4 pk = {pack_bf16(z[0].x, z[0].y), pack_bf16(z[1].x, z[1].y), pack_bf16(z[2].x, z[2].y), pack_bf16(z[3].x, z[3].y)};
      __builtin_amdgcn_raw_buffer_store_b128(pk, h1rs, fb + hf * 1024, 0, 16);
      own[it][hf] = pk;
      SCHED_FENCE();
    }
  }
  f32x16 acc2[4] = {};
#pragma unroll
  for (int ks = 0; ks < 2; ++ks) {
    const bf16x8 y0 = __builtin_bit_cast(bf16x8, own[0][ks]), y1 = __builtin_bit_cast(bf16x8, own[1][ks]);
    acc2[0] = __builtin_amdgcn_mfma_f32_32x32x16_bf16(a0[ks], y0, acc2[0], 0, 0, 0);
    acc2[1] = __builtin_amdgcn_mfma_f32_32x32x16_bf16(a0[ks], y1, acc2[1], 0, 0, 0);
    acc2[2] = __builtin_amdgcn_mfma_f32_32x32x16_bf16(a1[ks], y0, acc2[2], 0, 0, 0);
    acc2[3] = __builtin_amdgcn_mfma_f32_32x32x16_bf16(a1[ks], y1, acc2[3], 0, 0, 0);
  }
  wait_vm<0>();
  __builtin_amdgcn_s_barrier();
  asm volatile("" ::: "memory");
  gu32* cnt = (gu32*)(ws + OFF_CNT) + grp * 64;
  if (wv == 0) {
    if (lane == 0) {
      __hip_atomic_fetch_add(cnt, 1u, __ATOMIC_RELAXED, __HIP_MEMORY_SCOPE_AGENT);
      for (unsigned spins = 0; spins < (1u << 20); ++spins) {
        if (__hip_atomic_load(cnt, __ATOMIC_RELAXED, __HIP_MEMORY_SCOPE_AGENT) >= 7u) break;
        __builtin_amdgcn_s_sleep(1);
      }
    }
    __builtin_amdgcn_fence(__ATOMIC_ACQUIRE, "wavefront");
  }
  __builtin_amdgcn_s_barrier();
  __builtin_amdgcn_fence(__ATOMIC_ACQUIRE, "wavefront");
  asm volatile("" ::: "memory");
  const unsigned hb0 = (((grp * 2 + (wv & 1)) * 112) * 64 + lane) * 16, hb1 = (((grp * 2 + 1 - (wv & 1)) * 112) * 64 + lane) * 16;
  u32x4 b0[H1_WIN], b1v[H1_WIN];
#pragma unroll
  for (int i = 0; i < H1_WIN; ++i) {
    b0[i] = __builtin_amdgcn_raw_buffer_load_b128(h1rs, hb0, KSLOT(2 + i) * 1024, H1_AUX);
    b1v[i] = __builtin_amdgcn_raw_buffer_load_b128(h1rs, hb1, KSLOT(2 + i) * 1024, H1_AUX);
  }
  const int d0 = j * 64 + (t2 >> 1) * 32 + 16 * hf2 + 8 * hh;
  const u32x4 bz = *(const u32x4*)(b2 + d0);
  SCHED_FENCE();
#pragma unroll
  for (int ks = 0; ks < 12; ++ks) {
    const int sl = ks % H1_WIN;
    const bf16x8 y0 = __builtin_bit_cast(bf16x8, b0[sl]), y1 = __builtin_bit_cast(bf16x8, b1v[sl]);
    acc2[0] = __builtin_amdgcn_mfma_f32_32x32x16_bf16(a0[2 + ks], y0, acc2[0], 0, 0, 0);
    acc2[1] = __builtin_amdgcn_mfma_f32_32x32x16_bf16(a0[2 + ks], y1, acc2[1], 0, 0, 0);
    acc2[2] = __builtin_amdgcn_mfma_f32_32x32x16_bf16(a1[2 + ks], y0, acc2[2], 0, 0, 0);
    acc2[3] = __builtin_amdgcn_mfma_f32_32x32x16_bf16(a1[2 + ks], y1, acc2[3], 0, 0, 0);
    if (ks + H1_WIN < 12) {
      b0[sl] = __builtin_amdgcn_raw_buffer_load_b128(h1rs, hb0, KSLOT(2 + ks + H1_WIN) * 1024, H1_AUX);
      b1v[sl] = __builtin_amdgcn_raw_buffer_load_b128(h1rs, hb1, KSLOT(2 + ks + H1_WIN) * 1024, H1_AUX);
    }
    SCHED_FENCE();
  }
  f32x4* red = (f32x4*)smem;
  f32x4 own0 = {}, own1 = {};
#pragma unroll
  for (int a = 0; a < 8; ++a) {
    const int u = (a ^ (2 * (wv & 1)));
    const f32x4 v0 = {acc2[a >> 1][8 * (a & 1)], acc2[a >> 1][8 * (a & 1) + 1], acc2[a >> 1][8 * (a & 1) + 2], acc2[a >> 1][8 * (a & 1) + 3]};
    const f32x4 v1 = {acc2[a >> 1][8 * (a & 1) + 4], acc2[a >> 1][8 * (a & 1) + 5], acc2[a >> 1][8 * (a & 1) + 6], acc2[a >> 1][8 * (a & 1) + 7]};
    if (u == wv) { own0 = v0; own1 = v1; }
    else {
      const int slot = u * 7 + (wv > u ? wv - 1 : wv);
      red[(slot * 2) * 64 + lane] = v0;
      red[(slot * 2 + 1) * 64 + lane] = v1;
    }
  }
  __syncthreads();
  {
    f32x4 s0[7], s1[7];
#pragma unroll
    for (int i = 0; i < 7; ++i) { s0[i] = red[((wv * 7 + i) * 2) * 64 + lane]; s1[i] = red[((wv * 7 + i) * 2 + 1) * 64 + lane]; }
    const f32x4 o0 = ((own0 + s0[0]) + (s0[1] + s0[2])) + ((s0[3] + s0[4]) + (s0[5] + s0[6]));
    const f32x4 o1 = ((own1 + s1[0]) + (s1[1] + s1[2])) + ((s1[3] + s1[4]) + (s1[5] + s1[6]));
    const u32x4 hr = __builtin_bit_cast(u32x4, res);
    unsigned pk[4];
#pragma unroll
    for (int e = 0; e < 4; ++e) {
      const float x0 = (e < 2) ? o0[2 * e] : o1[2 * e - 4], x1 = (e < 2) ? o0[2 * e + 1] : o1[2 * e - 3];
      const unsigned ob_ = pack_bf16(x0, x1);
      const unsigned o2 = pack_bf16(bflo(ob_) + bflo(bz[e]), bfhi(ob_) + bfhi(bz[e]));
      pk[e] = pack_bf16(bflo(hr[e]) + bflo(o2), bfhi(hr[e]) + bfhi(o2));
    }
    const __amdgpu_buffer_rsrc_t ors = __builtin_amdgcn_make_buffer_rsrc((void*)out, 0, L_ * D_ * 2, 0x00020000);
    __builtin_amdgcn_raw_buffer_store_b128((u32x4){pk[0], pk[1], pk[2], pk[3]}, ors, ((tok0 + c2 * 32 + col) * D_ + d0) * 2, 0, OUT_AUX);
  }
}

extern "C" void kernel_launch(void* const* d_in, const int* in_sizes, int n_in, void* d_out, int out_size, void* d_ws,
                              size_t ws_size, hipStream_t stream) {
  const u16* x = (const u16*)d_in[0];
  const u16* lar = (const u16*)d_in[1];
  const u16* aim = (const u16*)d_in[2];
  const u16* cr = (const u16*)d_in[3];
  const u16* ci = (const u16*)d_in[4];
  const u16* ldt = (const u16*)d_in[5];
  const u16* dsk = (const u16*)d_in[6];
  const u16* w1 = (const u16*)d_in[8];
  const u16* b1 = (const u16*)d_in[9];
  const u16* w2 = (const u16*)d_in[10];
  const u16* b2 = (const u16*)d_in[11];
  char* ws = (char*)d_ws;
  k1_kernel<<<K1_GRID, 256, 0, stream>>>(x, lar, aim, cr, ci, ldt, w1, b1, w2, ws);
  k2_kernel<<<224, 512, 0, stream>>>(dsk, b2, ws, (u16*)d_out);
}
```

```cpp
#include <hip/hip_runtime.h>
#include <hip/hip_bf16.h>
#include <stdint.h>

typedef __attribute__((ext_vector_type(8))) __bf16 bf16x8;
typedef __attribute__((ext_vector_type(4))) __bf16 bf16x4;
typedef __attribute__((ext_vector_type(2))) __bf16 bf16x2;
typedef __attribute__((ext_vector_type(8))) _Float16 f16x8;
typedef __attribute__((ext_vector_type(2))) _Float16 f16x2;
typedef __attribute__((ext_vector_type(2))) float f32x2;
typedef __attribute__((ext_vector_type(4))) float f32x4;
typedef __attribute__((ext_vector_type(16))) float f32x16;
typedef __attribute__((ext_vector_type(4))) unsigned u32x4;
typedef __attribute__((ext_vector_type(2))) unsigned u32x2;
typedef unsigned short u16;

#define DI __device__ __forceinline__
#define SCHED_FENCE() __builtin_amdgcn_sched_barrier(0)

constexpr int L_ = 2048, D_ = 448, N_ = 256, F_ = 1792;
constexpr int KR_LEN = 2304;

constexpr size_t OFF_UT = 0;
constexpr size_t OFF_XT = OFF_UT + (size_t)D_ * L_ * 2;
constexpr size_t OFF_KR = OFF_XT + (size_t)D_ * L_ * 2;
constexpr size_t OFF_KS = OFF_KR + (size_t)D_ * KR_LEN * 2;
constexpr int W1_FRAGS = 29;
constexpr size_t OFF_CNT = OFF_KS + 2048;
constexpr size_t OFF_W1 = OFF_CNT + 32768;
constexpr size_t OFF_W2 = OFF_W1 + (size_t)56 * W1_FRAGS * 1024;
constexpr int HT_LD = L_ + 128;
constexpr size_t OFF_HT = OFF_W2 + (size_t)D_ * F_ * 2;
constexpr size_t OFF_H1 = OFF_HT + (size_t)D_ * HT_LD * 2;
constexpr size_t WS_END = OFF_H1 + (size_t)L_ * F_ * 2;

DI float bf2f(u16 v) { return __uint_as_float(((unsigned)v) << 16); }
DI float bflo(unsigned p) { return __uint_as_float(p << 16); }
DI float bfhi(unsigned p) { return __uint_as_float(p & 0xffff0000u); }
DI unsigned pack_bf16(float a, float b) {
  f32x2 v = {a, b};
  return __builtin_bit_cast(unsigned, __builtin_convertvector(v, bf16x2));
}
DI unsigned pack_f16(float a, float b) {
  f32x2 v = {a, b};
  return __builtin_bit_cast(unsigned, __builtin_convertvector(v, f16x2));
}
DI float rbf(float a) { return bflo(pack_bf16(a, a)); }
template <int CTRL> DI float dpp_add(float v) {
  const int t = __builtin_amdgcn_update_dpp(0, __float_as_int(v), CTRL, 0xf, 0xf, true);
  return v + __int_as_float(t);
}
DI float wave_sum(float v) {
  v = dpp_add<0xB1>(v);
  v = dpp_add<0x4E>(v);
  v = dpp_add<0x141>(v);
  v = dpp_add<0x140>(v);
  auto r = __builtin_amdgcn_permlane16_swap(__float_as_uint(v), __float_as_uint(v), false, false);
  v = __uint_as_float(r[0]) + __uint_as_float(r[1]);
  auto r2 = __builtin_amdgcn_permlane32_swap(__float_as_uint(v), __float_as_uint(v), false, false);
  return __uint_as_float(r2[0]) + __uint_as_float(r2[1]);
}
#define LN_AUX 2
#define OUT_AUX 0
DI __amdgpu_buffer_rsrc_t ws_rsrc(char* ws) { return __builtin_amdgcn_make_buffer_rsrc((void*)ws, 0, (int)WS_END, 0x00020000); }
DI void wst128(__amdgpu_buffer_rsrc_t rs, size_t off, u32x4 v) { __builtin_amdgcn_raw_buffer_store_b128(v, rs, (unsigned)off, 0, 16); }
DI f32x2 cswz(f32x2 b) { return b; }
DI f32x2 cm_b(f32x2 t, f32x2 a, f32x2 b) {
  f32x2 o;
  asm("v_pk_fma_f32 %0, %1, %2, %3 op_sel:[1,1,0] op_sel_hi:[1,0,1] neg_lo:[0,1,0]" : "=v"(o) : "v"(a), "v"(b), "v"(t));
  return o;
}
DI f32x2 cexp_p(float ar2, float air, float p) {
  const float mag = __builtin_amdgcn_exp2f(ar2 * p);
  const float ph = __builtin_amdgcn_fractf(air * p);
  return (f32x2){mag * __builtin_amdgcn_cosf(ph), mag * __builtin_amdgcn_sinf(ph)};
}
DI unsigned pk16(f32x2 v) { return __builtin_bit_cast(unsigned, __builtin_convertvector(v, f16x2)); }
DI void pow2_scale(float s, float& scale, float& inv) {
  int ex = (s > 0.f) ? __builtin_amdgcn_frexp_expf(s) : 14;
  ex = min(max(ex, -100), 120);
  scale = ldexpf(1.f, 14 - ex);
  inv = ldexpf(1.f, ex - 14);
}
DI int perm23(int r) { return (r & ~12) | ((r & 4) << 1) | ((r & 8) >> 1); }

constexpr int K1_KGEN = 448, K1_LN = 128, K1_WT = 112;
constexpr int K1_GRID = K1_KGEN + K1_LN + K1_WT;
constexpr int KG_RS = 272;
constexpr int KG_WAVE = 32 * KG_RS;
constexpr int KG_PART = 4 * KG_WAVE;
constexpr int K1_LDS = KG_PART + 4 * 48 * 16 * 4 + 64;

struct KTab { unsigned short v[2048]; };
constexpr KTab make_ktab() {
  KTab t{};
  for (int i = 0; i < 2048; ++i) {
    const int l = 2047 - i;
    int e = 0;
    while ((l >> (8 + e)) != 0) ++e;
    int p = l;
    if (e > 0) {
      const int q = l >> e, rem = l & ((1 << e) - 1), half = 1 << (e - 1);
      p = (q + ((rem > half || (rem == half && (q & 1))) ? 1 : 0)) << e;
    }
    int e2 = 0;
    while ((p >> (8 + e2)) != 0) ++e2;
    if (e2 > 3) e2 = 3;
    const int row = 8 * e2 + ((p >> 7) << (3 - e2)) + ((p & 7) >> e2);
    const int col = (p & 127) >> 3;
    t.v[i] = (unsigned short)(row * 16 + col);
  }
  return t;
}
__device__ const KTab KTAB = make_ktab();

DI void k1_kgen(char* smem, int d, const u16* __restrict__ lar_p, const u16* __restrict__ aim_p,
                const u16* __restrict__ cr_p, const u16* __restrict__ ci_p, const u16* __restrict__ ldt_p,
                char* __restrict__ ws) {
  const int tid = threadIdx.x, lane = tid & 63;
  const int kq = __builtin_amdgcn_readfirstlane(tid >> 6);
  const int idx = d * N_ + tid;
  const float lar = bf2f(lar_p[idx]), aim = bf2f(aim_p[idx]), cr = bf2f(cr_p[idx]), ci = bf2f(ci_p[idx]);
  const float ldt = bf2f(ldt_p[d]);
  const u32x4 tab = *(const u32x4*)(&KTAB.v[tid * 8]);
  const float LOG2E = 1.4426950408889634f;
  const float dt = rbf(__builtin_amdgcn_exp2f(ldt * LOG2E));
  const float are = -rbf(__builtin_amdgcn_exp2f(lar * LOG2E));
  const float ar = are * dt, ai = aim * dt;
  const float Cr = cr * dt, Ci = ci * dt;
  float* red = (float*)(smem + K1_LDS - 64);
  const float sw = wave_sum(2.f * (fabsf(Cr) + fabsf(Ci)));
  float wscale, winv;
  pow2_scale(sw, wscale, winv);
  if (lane == 0) red[kq] = sw;
  const float ar2 = ar * LOG2E, air = ai * 0.15915494309189535f;
  char* wb = smem + kq * KG_WAVE + lane * 4;
  char* hb = wb + 16 * KG_RS;
  const int r16 = lane & 15, gq = lane >> 4;
  const char* base = smem + kq * KG_WAVE + r16 * KG_RS + gq * 16;
  f32x4 acc[3] = {};
  f16x8 bfr[4];
#define CM_A(t, a, b) const f32x2 t = (f32x2){(a).x, (a).x} * (b)
#define CM_B(t, a, bs) cm_b(t, a, bs)
  f32x2 Lo[16], g[8], R[8], z[9];
  const f32x2 E1 = cexp_p(ar2, air, 1.f);
  Lo[1] = cexp_p(ar2, -air, 8.f);
  R[1] = cexp_p(ar2, air, 128.f);
  g[0] = (f32x2){2.f * wscale * Cr, 2.f * wscale * Ci};
  const f32x2 g0s = cswz(g[0]), R1s = cswz(R[1]);
  f32x2 E2, E4, R8, R16;
  {
    const f32x2 E1s = cswz(E1), L1s = cswz(Lo[1]);
    CM_A(t0, E1, E1); CM_A(t1, Lo[1], Lo[1]); CM_A(t2, R[1], R[1]); CM_A(t3, E1, g[0]);
    SCHED_FENCE();
    E2 = CM_B(t0, E1, E1s); Lo[2] = CM_B(t1, Lo[1], L1s); R[2] = CM_B(t2, R[1], R1s); g[1] = CM_B(t3, E1, g0s);
    SCHED_FENCE();
  }
  const f32x2 E2s = cswz(E2), L2s = cswz(Lo[2]), R2s = cswz(R[2]);
  {
    CM_A(t0, E2, E2); CM_A(t1, Lo[2], Lo[2]); CM_A(t2, R[2], R[2]); CM_A(t3, g[0], E2); CM_A(t4, g[1], E2);
    CM_A(t5, Lo[1], Lo[2]); CM_A(t6, R[1], R[2]);
    SCHED_FENCE();
    E4 = CM_B(t0, E2, E2s); Lo[4] = CM_B(t1, Lo[2], L2s); R[4] = CM_B(t2, R[2], R2s); g[2] = CM_B(t3, g[0], E2s);
    g[3] = CM_B(t4, g[1], E2s); Lo[3] = CM_B(t5, Lo[1], L2s); R[3] = CM_B(t6, R[1], R2s);
    SCHED_FENCE();
  }
  const f32x2 E4s = cswz(E4), L4s = cswz(Lo[4]), R4s = cswz(R[4]);
  {
    f32x2 t[12];
#pragma unroll
    for (int k = 0; k < 4; ++k) {
      t[k] = (f32x2){g[k].x, g[k].x} * E4;
      t[4 + k] = (f32x2){Lo[k + 1].x, Lo[k + 1].x} * Lo[4];
      t[8 + k] = (f32x2){R[k + 1].x, R[k + 1].x} * R[4];
    }
    SCHED_FENCE();
#pragma unroll
    for (int k = 0; k < 4; ++k) {
      g[4 + k] = CM_B(t[k], g[k], E4s);
      Lo[5 + k] = CM_B(t[4 + k], Lo[k + 1], L4s);
      if (k < 3) R[5 + k] = CM_B(t[8 + k], R[k + 1], R4s); else R8 = CM_B(t[8 + k], R[4], R4s);
    }
    SCHED_FENCE();
  }
  {
    const f32x2 L8s = cswz(Lo[8]), R8s = cswz(R8), R3s = cswz(R[3]), g4s = cswz(g[4]);
    f32x2 t[33];
#pragma unroll
    for (int k = 1; k < 8; ++k) t[24 + k] = (f32x2){Lo[k].x, Lo[k].x} * Lo[8];
    t[24] = (f32x2){g[0].x, g[0].x} * R8;
    t[32] = (f32x2){R8.x, R8.x} * R8;
#pragma unroll
    for (int c = 0; c < 8; ++c) t[c] = (f32x2){g[c].x, g[c].x} * R[1];
#pragma unroll
    for (int c = 0; c < 4; ++c) { t[8 + c] = (f32x2){g[2 * c].x, g[2 * c].x} * R[2]; t[12 + c] = (f32x2){g[2 * c].x, g[2 * c].x} * R[3]; }
#pragma unroll
    for (int h = 0; h < 4; ++h) { t[16 + 2 * h] = (f32x2){R[4 + h].x, R[4 + h].x} * g[0]; t[17 + 2 * h] = (f32x2){R[4 + h].x, R[4 + h].x} * g[4]; }
    SCHED_FENCE();
    z[0] = CM_B(t[24], g[0], R8s);
    R16 = CM_B(t[32], R8, R8s);
#pragma unroll
    for (int k = 1; k < 8; ++k) Lo[8 + k] = CM_B(t[24 + k], Lo[k], L8s);
#pragma unroll
    for (int c = 0; c < 8; ++c) t[c] = CM_B(t[c], g[c], R1s);
#pragma unroll
    for (int c = 0; c < 4; ++c) { t[8 + c] = CM_B(t[8 + c], g[2 * c], R2s); t[12 + c] = CM_B(t[12 + c], g[2 * c], R3s); }
#pragma unroll
    for (int h = 0; h < 4; ++h) { t[16 + 2 * h] = CM_B(t[16 + 2 * h], R[4 + h], g0s); t[17 + 2 * h] = CM_B(t[17 + 2 * h], R[4 + h], g4s); }
    SCHED_FENCE();
    *(unsigned*)(wb) = 0x00003c00u;
#pragma unroll
    for (int k = 1; k < 16; ++k) *(unsigned*)(wb + k * KG_RS) = pk16(Lo[k]);
#pragma unroll
    for (int c = 0; c < 8; ++c) *(unsigned*)(hb + c * KG_RS) = pk16(g[c]);
#pragma unroll
    for (int i = 0; i < 8; ++i) *(unsigned*)(hb + (8 + i) * KG_RS) = pk16(t[i]);
#pragma unroll
    for (int ks = 0; ks < 4; ++ks) {
      bfr[ks] = *(const f16x8*)(base + ks * 64);
      acc[0] = __builtin_amdgcn_mfma_f32_16x16x32_f16(*(const f16x8*)(base + 16 * KG_RS + ks * 64), bfr[ks], acc[0], 0, 0, 0);
    }
#pragma unroll
    for (int i = 0; i < 16; ++i) *(unsigned*)(hb + i * KG_RS) = pk16(t[8 + i]);
#pragma unroll
    for (int ks = 0; ks < 4; ++ks)
      acc[1] = __builtin_amdgcn_mfma_f32_16x16x32_f16(*(const f16x8*)(base + 16 * KG_RS + ks * 64), bfr[ks], acc[1], 0, 0, 0);
    SCHED_FENCE();
  }
  {
    const f32x2 z0s = cswz(z[0]);
    f32x2 t[9];
#pragma unroll
    for (int k = 1; k < 8; ++k) t[k] = (f32x2){R[k].x, R[k].x} * z[0];
    t[8] = (f32x2){R16.x, R16.x} * g[0];
    SCHED_FENCE();
#pragma unroll
    for (int k = 1; k < 8; ++k) z[k] = CM_B(t[k], R[k], z0s);
    z[8] = CM_B(t[8], R16, g0s);
    SCHED_FENCE();
#pragma unroll
    for (int h = 0; h < 9; ++h) *(unsigned*)(hb + h * KG_RS) = pk16(z[h]);
#pragma unroll
    for (int ks = 0; ks < 4; ++ks)
      acc[2] = __builtin_amdgcn_mfma_f32_16x16x32_f16(*(const f16x8*)(base + 16 * KG_RS + ks * 64), bfr[ks], acc[2], 0, 0, 0);
  }
  {
    float* part = (float*)(smem + KG_PART) + kq;
#pragma unroll
    for (int m = 0; m < 3; ++m)
#pragma unroll
      for (int j = 0; j < 4; ++j) part[((16 * m + 4 * gq + j) * 16 + r16) * 4] = acc[m][j] * winv;
  }
  __syncthreads();
  {
    float scale, inv_scale;
    pow2_scale((red[0] + red[1]) + (red[2] + red[3]), scale, inv_scale);
    const f32x4* part = (const f32x4*)(smem + KG_PART);
    unsigned pk[4];
#pragma unroll
    for (int j = 0; j < 4; ++j) {
      const f32x4 p0 = part[tab[j] & 0xffffu], p1 = part[tab[j] >> 16];
      pk[j] = pack_f16(((p0[0] + p0[1]) + (p0[2] + p0[3])) * scale, ((p1[0] + p1[1]) + (p1[2] + p1[3])) * scale);
    }
    const __amdgpu_buffer_rsrc_t rs = ws_rsrc(ws);
    const size_t kr = OFF_KR + (size_t)d * KR_LEN * 2;
    wst128(rs, kr + tid * 16, (u32x4){pk[0], pk[1], pk[2], pk[3]});
    if (tid < 32) wst128(rs, kr + 4096 + tid * 16, (u32x4){0u, 0u, 0u, 0u});
    if (tid == 0) ((float*)(ws + OFF_KS))[d] = inv_scale;
  }
}

DI float reduce4(const float (&a)[4]) {
  float v[2];
#pragma unroll
  for (int i = 0; i < 2; ++i) {
    auto r = __builtin_amdgcn_permlane32_swap(__float_as_uint(a[i]), __float_as_uint(a[i + 2]), false, false);
    v[i] = __uint_as_float(r[0]) + __uint_as_float(r[1]);
  }
  auto r = __builtin_amdgcn_permlane16_swap(__float_as_uint(v[0]), __float_as_uint(v[1]), false, false);
  float x = __uint_as_float(r[0]) + __uint_as_float(r[1]);
  x = dpp_add<0xB1>(x);
  x = dpp_add<0x4E>(x);
  x = dpp_add<0x141>(x);
  x = dpp_add<0x140>(x);
  return x;
}
DI float bcast_row(float w, int t) { return __uint_as_float(__builtin_amdgcn_readlane(__float_as_uint(w), 16 * t)); }

constexpr int LN_PL = 448 * 32 + 56 * 16;
DI void k1_ln(char* smem, int blk, const u16* __restrict__ x, char* __restrict__ ws) {
  const int tid = threadIdx.x, lane = tid & 63, wv = tid >> 6;
  const int tok0 = blk * 16 + wv * 4;
  const bool act = lane < 56;
  u32x4 xr[4];
#pragma unroll
  for (int t = 0; t < 4; ++t) {
    xr[t] = (u32x4){0u, 0u, 0u, 0u};
    if (act) xr[t] = *(const u32x4*)(x + (size_t)(tok0 + t) * D_ + lane * 8);
  }
  f32x2 xf[4][4];
  float part[4];
#pragma unroll
  for (int t = 0; t < 4; ++t) {
#pragma unroll
    for (int j = 0; j < 4; ++j) xf[t][j] = (f32x2){bflo(xr[t][j]), bfhi(xr[t][j])};
    const f32x2 s2 = (xf[t][0] + xf[t][1]) + (xf[t][2] + xf[t][3]);
    part[t] = s2.x + s2.y;
  }
  const float wm = reduce4(part) * (1.f / D_);
#pragma unroll
  for (int t = 0; t < 4; ++t) {
    const float m = bcast_row(wm, t);
    f32x2 q2 = {0.f, 0.f};
#pragma unroll
    for (int j = 0; j < 4; ++j) { const f32x2 dd = xf[t][j] - (f32x2){m, m}; q2 = __builtin_elementwise_fma(dd, dd, q2); }
    part[t] = act ? q2.x + q2.y : 0.f;
  }
  const float wq = reduce4(part);
  const float sd = rbf(sqrtf(rbf(rbf(wq * (1.f / D_)) + 1e-5f)));
  const float wr = 1.f / sd;
  const float wb = rbf(wm);
  f32x2 un[4][4];
#pragma unroll
  for (int t = 0; t < 4; ++t) {
    const float mb = bcast_row(wb, t), ri = bcast_row(wr, t);
#pragma unroll
    for (int j = 0; j < 4; ++j) {
      const f32x2 dd = xf[t][j] - (f32x2){mb, mb};
      const unsigned pb = pack_bf16(dd.x, dd.y);
      un[t][j] = (f32x2){bflo(pb), bfhi(pb)} * (f32x2){ri, ri};
    }
  }
  if (act) {
    char* ub = smem + lane * (8 * 32 + 16) + wv * 8;
#pragma unroll
    for (int j = 0; j < 8; ++j) {
      u32x2 pu, px;
#pragma unroll
      for (int t = 0; t < 2; ++t) {
        pu[t] = pack_bf16(un[2 * t][j >> 1][j & 1], un[2 * t + 1][j >> 1][j & 1]);
        const unsigned a = xr[2 * t][j >> 1], b = xr[2 * t + 1][j >> 1];
        px[t] = (j & 1) ? __builtin_amdgcn_perm(b, a, 0x07060302u) : __builtin_amdgcn_perm(b, a, 0x05040100u);
      }
      *(u32x2*)(ub + j * 32) = pu;
      *(u32x2*)(ub + LN_PL + j * 32) = px;
    }
  }
  __syncthreads();
  const __amdgpu_buffer_rsrc_t wrs = ws_rsrc(ws);
#pragma unroll
  for (int i = 0; i < 4; ++i) {
    const int ch = i * 128 + (tid >> 1), v = tid & 1;
    if (ch < D_) {
      const int o = ch * 32 + (ch >> 3) * 16 + v * 16;
      const u32x4 a = *(const u32x4*)(smem + o);
      const u32x4 b = *(const u32x4*)(smem + LN_PL + o);
      __builtin_amdgcn_raw_buffer_store_b128(a, wrs, (unsigned)(OFF_UT + ((size_t)ch * L_ + blk * 16 + v * 8) * 2), 0, LN_AUX);
      __builtin_amdgcn_raw_buffer_store_b128(b, wrs, (unsigned)(OFF_XT + ((size_t)ch * L_ + blk * 16 + v * 8) * 2), 0, LN_AUX);
    }
  }
}

typedef __attribute__((address_space(3))) bf16x4* lds_bf16x4_p;
DI void k1_wtile(char* smem, const u16* __restrict__ src, int ldn, size_t dsto, const u16* __restrict__ b1s, char* ws) {
  const int tid = threadIdx.x, lane = tid & 63, wv = tid >> 6;
  const __amdgpu_buffer_rsrc_t rs = ws_rsrc(ws);
  u32x4 v[7];
#pragma unroll
  for (int i = 0; i < 7; ++i) {
    const int c = i * 256 + tid, k = c >> 2, q = c & 3;
    v[i] = *(const u32x4*)(src + (size_t)k * ldn + q * 8);
  }
  unsigned b1v = 0;
  if (b1s) b1v = b1s[perm23(tid & 31)];
  SCHED_FENCE();
  {
    const int q = tid & 3;
    char* dst = smem + (tid >> 2) * 64 + 32 * (q >> 1) + 8 * (q & 1);
#pragma unroll
    for (int i = 0; i < 7; ++i) {
      *(u32x2*)(dst + i * 4096) = (u32x2){v[i][0], v[i][1]};
      *(u32x2*)(dst + i * 4096 + 16) = (u32x2){v[i][2], v[i][3]};
    }
  }
  __syncthreads();
  {
    const int hh = lane >> 5, blk = (lane >> 4) & 1, q = (lane & 15) >> 2, p = lane & 3;
    const char* rd = smem + (8 * hh + q) * 64 + 32 * blk + 8 * p + wv * 1024;
#pragma unroll
    for (int i = 0; i < 7; ++i) {
      const bf16x4 lo = __builtin_amdgcn_ds_read_tr16_b64_v4bf16((lds_bf16x4_p)(rd + i * 4096));
      const bf16x4 hi = __builtin_amdgcn_ds_read_tr16_b64_v4bf16((lds_bf16x4_p)(rd + i * 4096 + 256));
      const bf16x8 f = __builtin_shufflevector(lo, hi, 0, 1, 2, 3, 4, 5, 6, 7);
      wst128(rs, dsto + (size_t)((i * 4 + wv) * 64 + lane) * 16, __builtin_bit_cast(u32x4, f));
    }
  }
  if (b1s) {
    const int cp = tid & 15, kg = tid >> 4;
    f32x2 sacc = {0.f, 0.f};
#pragma unroll
    for (int k = 0; k < 28; ++k) {
      const unsigned w = *(const unsigned*)(smem + (kg * 28 + k) * 64 + cp * 4);
      sacc += (f32x2){bflo(w), bfhi(w)};
    }
    f32x2* red = (f32x2*)(smem + 448 * 64);
    red[tid] = sacc;
    __syncthreads();
    if (tid < 64) {
      const float* redf = (const float*)red;
      float a = 0.f;
#pragma unroll
      for (int g = 0; g < 16; ++g) a += redf[g * 32 + (tid & 31)];
      const float hi = rbf(a);
      const unsigned w0 = pack_bf16(hi, a - hi);
      wst128(rs, dsto + (size_t)(28 * 64 + tid) * 16, (u32x4){tid < 32 ? w0 : 0u, tid < 32 ? b1v : 0u, 0u, 0u});
    }
  }
}

#define CODE_PF1 166
#define CODE_PF2 88
__global__ __launch_bounds__(256, 3) void k1_kernel(const u16* __restrict__ x, const u16* __restrict__ lar,
                                                    const u16* __restrict__ aim, const u16* __restrict__ cr,
                                                    const u16* __restrict__ ci, const u16* __restrict__ ldt,
                                                    const u16* __restrict__ w1, const u16* __restrict__ b1,
                                                    const u16* __restrict__ w2, char* __restrict__ ws) {
  __shared__ __attribute__((aligned(16))) char smem[K1_LDS];
  int b = blockIdx.x;
  if (b < K1_LN) {
    unsigned pf = 0;
    if (b < 16 && threadIdx.x < CODE_PF1) pf = *(const unsigned*)((__builtin_amdgcn_s_getpc() & ~(uint64_t)3) - 6144 + threadIdx.x * 128);
    if (b == 0 && threadIdx.x < 65) ((unsigned*)(ws + OFF_CNT))[threadIdx.x * 64] = 0u;
    k1_ln(smem, b, x, ws);
    asm volatile("" ::"v"(pf));
    return;
  }
  b -= K1_LN;
  if (b < K1_KGEN) { k1_kgen(smem, b, lar, aim, cr, ci, ldt, ws); return; }
  b -= K1_KGEN;
  if (b < 56) {
    k1_wtile(smem, w1 + b * 32, F_, OFF_W1 + (size_t)b * W1_FRAGS * 1024, b1 + b * 32, ws);
  } else {
    const int dt = (b - 56) >> 2, kc = (b - 56) & 3;
    k1_wtile(smem, w2 + (size_t)kc * 448 * D_ + dt * 32, D_, OFF_W2 + ((size_t)dt * 112 + kc * 28) * 1024, nullptr, ws);
  }
}

constexpr int CV_CS = 4160;
constexpr int CV_KOFF = 8 * CV_CS;
constexpr int CV_CH = CV_KOFF + KR_LEN * 2;
constexpr int K3_TILE = 448 * 128;
constexpr int K3_RED = 8 * 7 * 2048;
constexpr int K3_STAT = K3_RED;
constexpr int K3_LDS = 2 * CV_CH + 2 * 24576;
static_assert(K3_STAT + 4 * 64 * 8 <= K3_LDS && (CV_CH & 15) == 0, "lds");
typedef __attribute__((address_space(1))) unsigned gu32;

DI bf16x8 k3_bfrag(const char* smem, unsigned lane_off0, unsigned lane_off1, int off) {
  const bf16x4 lo = __builtin_amdgcn_ds_read_tr16_b64_v4bf16((lds_bf16x4_p)(smem + lane_off0 + off));
  const bf16x4 hi = __builtin_amdgcn_ds_read_tr16_b64_v4bf16((lds_bf16x4_p)(smem + lane_off1 + off));
  return __builtin_shufflevector(lo, hi, 0, 1, 2, 3, 4, 5, 6, 7);
}
#define W1_EARLY 5
#define H1_AUX 16
#define POLL_SLEEP 1
#define H1_WIN 6
template <int N> DI void wait_vm() { asm volatile("s_waitcnt vmcnt(%0)" ::"i"(N) : "memory"); }

__global__ __launch_bounds__(512, 2) void k2_kernel(const u16* __restrict__ dskip, const u16* __restrict__ b2,
                                                    char* __restrict__ ws, u16* __restrict__ out) {
  __shared__ __attribute__((aligned(16))) char smem[K3_LDS];
  const int tid = threadIdx.x, lane = tid & 63;
  const int wv = __builtin_amdgcn_readfirstlane(tid >> 6);
  const int xcd = blockIdx.x & 7, idx = blockIdx.x >> 3;
  const int grp = 4 * xcd + idx / 7, j = idx % 7;
  const int tok0 = grp * 64, ft = j * 8 + wv;
  const int sh = (wv >> 1) & 1;
  const int s2 = wv >> 2;
  unsigned pf = 0;
  if (tid >= 384 && tid < 384 + CODE_PF2) pf = *(const unsigned*)((__builtin_amdgcn_s_getpc() & ~(uint64_t)3) + (tid - 384) * 128);
  const __amdgpu_buffer_rsrc_t wrs = ws_rsrc(ws);
  const bf16x8* w1f = (const bf16x8*)(ws + OFF_W1) + (size_t)ft * W1_FRAGS * 64 + lane;
  const bf16x8* w1a = w1f + sh * 128;
  const bf16x8* w1b = w1f + (1 - sh) * 128;
  bf16x8 A[28];
#define ISSUEA(c) do { A[4 * (c)] = w1a[(4 * (c)) * 64]; A[4 * (c) + 1] = w1a[(4 * (c) + 1) * 64]; \
    A[4 * (c) + 2] = w1b[(4 * (c)) * 64]; A[4 * (c) + 3] = w1b[(4 * (c) + 1) * 64]; } while (0)
#define LDA(n) ((((n) & 2) == 0) ? w1a[(4 * ((n) >> 2) + ((n) & 1)) * 64] : w1b[(4 * ((n) >> 2) + ((n) & 1)) * 64])
  bf16x8 Aext;
  {
    const int d = 2 * blockIdx.x + (wv >> 2), cw = wv & 3, m0 = tid & 255;
    char* cs = smem + (wv >> 2) * CV_CH;
    const u16* uT = (const u16*)(ws + OFF_UT) + (size_t)d * L_;
    const u16* xT = (const u16*)(ws + OFF_XT) + (size_t)d * L_;
    const char* kr = ws + OFF_KR + (size_t)d * KR_LEN * 2;
    u32x4 c0 = {0u, 0u, 0u, 0u};
    if (m0 > 0) c0 = *(const u32x4*)(uT + (m0 - 1) * 8);
    const u32x4 c1 = *(const u32x4*)(uT + m0 * 8);
    const u32x4 k0 = *(const u32x4*)(kr + m0 * 16);
    u32x4 k1 = {0u, 0u, 0u, 0u};
    if (m0 < 32) k1 = *(const u32x4*)(kr + 4096 + m0 * 16);
    const int c16 = lane & 15, g4 = lane >> 4;
    u32x2 ue[2], xe[2];
#pragma unroll
    for (int i = 0; i < 2; ++i) {
      const int pp = 256 * (2 * cw + i) + 16 * c16 + 4 * g4;
      ue[i] = *(const u32x2*)(uT + pp);
      xe[i] = *(const u32x2*)(xT + pp);
    }
    const float inv_scale = ((const float*)(ws + OFF_KS))[d];
    const float dsk = bf2f(dskip[d]);
    SCHED_FENCE();
    *(u32x4*)(cs + CV_KOFF + m0 * 16) = k0;
    if (m0 < 32) *(u32x4*)(cs + CV_KOFF + 4096 + m0 * 16) = k1;
    if (m0 < 8) *(u32x4*)(cs + m0 * CV_CS) = (u32x4){0u, 0u, 0u, 0u};
    {
      unsigned pq[8];
#pragma unroll
      for (int e = 0; e < 4; ++e) {
        pq[e] = pack_f16(bflo(c0[e]), bfhi(c0[e]));
        pq[4 + e] = pack_f16(bflo(c1[e]), bfhi(c1[e]));
      }
      unsigned qq[7];
#pragma unroll
      for (int i = 0; i < 7; ++i) qq[i] = __builtin_amdgcn_alignbit(pq[i + 1], pq[i], 16);
      char* dst = cs + (m0 + 1) * 16;
#pragma unroll
      for (int rho = 0; rho < 8; ++rho) {
        const int k = (rho + 1) >> 1;
        const u32x4 w = (rho & 1) ? (u32x4){pq[k], pq[k + 1], pq[k + 2], pq[k + 3]} : (u32x4){qq[k], qq[k + 1], qq[k + 2], qq[k + 3]};
        *(u32x4*)(dst + rho * CV_CS) = w;
      }
    }
    __syncthreads();
    f32x4 cacc[8] = {};
    const char* abase = cs + (c16 & 7) * CV_CS + ((c16 & 8) + 8 * g4) * 2;
    const char* bbase = cs + CV_KOFF + (2032 - 16 * c16 + 8 * g4) * 2;
#pragma unroll
    for (int si = 0; si < 2; ++si) {
      const int sg = 2 * cw + si;
      f16x8 CA[8], CB[8];
#pragma unroll
      for (int i = 0; i < 8; ++i) {
        CA[i] = *(const f16x8*)(abase + sg * 64 + i * 512);
        CB[i] = *(const f16x8*)(bbase + sg * 64 - i * 512);
      }
#pragma unroll
      for (int t = 0; t < 8; ++t) {
#pragma unroll
        for (int ia = 0; ia <= t; ++ia)
          cacc[t] = __builtin_amdgcn_mfma_f32_16x16x32_f16(CA[ia], CB[t - ia], cacc[t], 0, 0, 0);
        if (t >= 3) {
          const int n0 = (si * 5 + (t - 3)) * 2;
          if (n0 < 4 * W1_EARLY) { A[n0] = LDA(n0); A[n0 + 1] = LDA(n0 + 1); }
          SCHED_FENCE();
        }
      }
      SCHED_FENCE();
    }
    f32x4* red = (f32x4*)(smem + 2 * CV_CH + (wv >> 2) * 24576);
    f32x4 own[2] = {};
#pragma unroll
    for (int t = 0; t < 8; ++t) {
      if ((t >> 1) == cw) own[t & 1] = cacc[t];
      else red[(t * 3 + (cw > (t >> 1) ? cw - 1 : cw)) * 64 + lane] = cacc[t];
    }
    __syncthreads();
#pragma unroll
    for (int i = 0; i < 2; ++i) {
      const int t = 2 * cw + i;
      const f32x4 sum = (own[i] + red[(t * 3) * 64 + lane]) + (red[(t * 3 + 1) * 64 + lane] + red[(t * 3 + 2) * 64 + lane]);
      float h[4];
#pragma unroll
      for (int e = 0; e < 4; ++e) {
        const float uu = (e & 1) ? bfhi(ue[i][e >> 1]) : bflo(ue[i][e >> 1]);
        const float xx = (e & 1) ? bfhi(xe[i][e >> 1]) : bflo(xe[i][e >> 1]);
        const float y = rbf(sum[e] * inv_scale);
        const float tt = rbf(y + rbf(uu * dsk));
        h[e] = xx + tt;
      }
      __builtin_amdgcn_raw_buffer_store_b64((u32x2){pack_bf16(h[0], h[1]), pack_bf16(h[2], h[3])}, wrs,
                                            (unsigned)(OFF_HT + ((size_t)d * HT_LD + 256 * t + 16 * c16 + 4 * g4) * 2), 0, 16);
    }
  }
  asm volatile("" ::"v"(pf));
  wait_vm<0>();
  __builtin_amdgcn_s_barrier();
  asm volatile("" ::: "memory");
  if (wv == 0) {
    gu32* gc = (gu32*)(ws + OFF_CNT) + 32 * 64;
    unsigned last = 0;
    if (lane == 0) {
      if (__hip_atomic_fetch_add(gc + (blockIdx.x & 15) * 64, 1u, __ATOMIC_RELAXED, __HIP_MEMORY_SCOPE_AGENT) == 13u)
        last = __hip_atomic_fetch_add(gc + 16 * 64, 1u, __ATOMIC_RELAXED, __HIP_MEMORY_SCOPE_AGENT) == 15u;
    }
    if (__builtin_amdgcn_readfirstlane(last)) {
      if (lane < 16) __hip_atomic_store(gc + (17 + lane) * 64, 1u, __ATOMIC_RELAXED, __HIP_MEMORY_SCOPE_AGENT);
    } else if (lane == 0) {
      for (unsigned spins = 0; spins < (1u << 20); ++spins) {
        if (__hip_atomic_load(gc + (17 + (blockIdx.x & 15)) * 64, __ATOMIC_RELAXED, __HIP_MEMORY_SCOPE_AGENT) != 0u) break;
        __builtin_amdgcn_s_sleep(POLL_SLEEP);
      }
    }
    __builtin_amdgcn_fence(__ATOMIC_ACQUIRE, "wavefront");
  }
  SCHED_FENCE();
#pragma unroll
  for (int c = W1_EARLY; c < 7; ++c) ISSUEA(c);
  Aext = w1f[28 * 64];
  SCHED_FENCE();
  __builtin_amdgcn_s_barrier();
  __builtin_amdgcn_fence(__ATOMIC_ACQUIRE, "wavefront");
  asm volatile("" ::: "memory");
  const unsigned hsrc = (unsigned)(OFF_HT + ((size_t)(tid >> 3) * HT_LD + tok0 + (tid & 7) * 8) * 2);
  const __amdgpu_buffer_rsrc_t w2rs = __builtin_amdgcn_make_buffer_rsrc((void*)(ws + OFF_W2), 0, D_ * F_ * 2, 0x00020000);
  const unsigned w2v = ((2 * j) * 112 * 64 + lane) * 16;
#define KSLOT(sl) ((sl) < 2 ? 16 * j + 2 * wv + (sl) : (12 * wv + (sl) - 2) + ((12 * wv + (sl) - 2) >= 16 * j ? 16 : 0))
#define LDW2(sl, dt) __builtin_bit_cast(bf16x8, __builtin_amdgcn_raw_buffer_load_b128(w2rs, w2v + (dt) * 112 * 1024, KSLOT(sl) * 1024, 0))
  u32x4 st[7];
  bf16x8 a0[14], a1[14];
#define ISSUE2(c) do { a0[2 * (c)] = LDW2(2 * (c), 0); a1[2 * (c)] = LDW2(2 * (c), 1); \
    a0[2 * (c) + 1] = LDW2(2 * (c) + 1, 0); a1[2 * (c) + 1] = LDW2(2 * (c) + 1, 1); } while (0)
#pragma unroll
  for (int c = 0; c < 7; ++c) st[c] = __builtin_amdgcn_raw_buffer_load_b128(wrs, hsrc, 64 * c * HT_LD * 2, 16);
  SCHED_FENCE();
  const int hh = lane >> 5, col = lane & 31;
  const unsigned q = (lane & 15) >> 2, p = lane & 3, blk = (lane >> 4) & 1;
  char* lw = smem + 128 * (tid >> 3) + 16 * ((tid & 7) ^ (((tid >> 4) & 1) << 2));
  unsigned oa[2][2], ob[2][2];
  {
    const unsigned r0 = 8 * hh + q, r1 = 8 * hh + 4 + q, sw = ((q >> 1) & 1) << 2;
#pragma unroll
    for (int it = 0; it < 2; ++it) {
      const unsigned c = (wv & 1) ^ it;
      const unsigned chk = (4 * c + 2 * blk + (p >> 1)) ^ sw;
      const unsigned x0 = 128 * r0 + 16 * chk + 8 * (p & 1), x1 = 128 * r1 + 16 * chk + 8 * (p & 1);
      oa[it][0] = x0 + sh * 4096; oa[it][1] = x1 + sh * 4096;
      ob[it][0] = x0 + (1 - sh) * 4096; ob[it][1] = x1 + (1 - sh) * 4096;
    }
  }
  f32x16 acc[2] = {};
  float ssum = 0.f, ssq = 0.f;
  const bf16x2 ones2 = {(__bf16)1.0f, (__bf16)1.0f};
#define WRITE1(c) do { *(u32x4*)(lw + (c) * 8192) = st[c]; } while (0)
#define RDB(n, it) ((((n) & 2) == 0) ? k3_bfrag(smem, oa[it][0], oa[it][1], ((n) >> 2) * 8192 + ((n) & 1) * 2048) \
                                      : k3_bfrag(smem, ob[it][0], ob[it][1], ((n) >> 2) * 8192 + ((n) & 1) * 2048))
  bf16x8 Bf[3][2];
#pragma unroll
  for (int c = 0; c < 7; ++c) WRITE1(c);
  __syncthreads();
  Bf[0][0] = RDB(0, 0); Bf[0][1] = RDB(0, 1); Bf[1][0] = RDB(1, 0); Bf[1][1] = RDB(1, 1);
#pragma unroll
  for (int n = 0; n < 28; ++n) {
    const int c = n >> 2, i = n & 3;
    acc[0] = __builtin_amdgcn_mfma_f32_32x32x16_bf16(A[n], Bf[n % 3][0], acc[0], 0, 0, 0);
    if (n + 2 < 28) Bf[(n + 2) % 3][0] = RDB(n + 2, 0);
    if (i < 2) {
      const u32x4 B = __builtin_bit_cast(u32x4, Bf[n % 3][0]);
      const bf16x2 pr = __builtin_bit_cast(bf16x2, s2 ? B[2] : B[0]);
      ssum = __builtin_amdgcn_fdot2_f32_bf16(pr, ones2, ssum, false);
      ssq = __builtin_amdgcn_fdot2_f32_bf16(pr, pr, ssq, false);
    }
    SCHED_FENCE();
    acc[1] = __builtin_amdgcn_mfma_f32_32x32x16_bf16(A[n], Bf[n % 3][1], acc[1], 0, 0, 0);
    if (n + 2 < 28) Bf[(n + 2) % 3][1] = RDB(n + 2, 1);
    if (i < 2) {
      const u32x4 B = __builtin_bit_cast(u32x4, Bf[n % 3][0]);
      const bf16x2 pr = __builtin_bit_cast(bf16x2, s2 ? B[3] : B[1]);
      ssum = __builtin_amdgcn_fdot2_f32_bf16(pr, ones2, ssum, false);
      ssq = __builtin_amdgcn_fdot2_f32_bf16(pr, pr, ssq, false);
    }
    if (i == 3) ISSUE2(c);
    SCHED_FENCE();
  }
  float2* stats = (float2*)(smem + K3_STAT);
  {
    auto r1 = __builtin_amdgcn_permlane32_swap(__float_as_uint(ssum), __float_as_uint(ssum), false, false);
    ssum = __uint_as_float(r1[0]) + __uint_as_float(r1[1]);
    auto r2 = __builtin_amdgcn_permlane32_swap(__float_as_uint(ssq), __float_as_uint(ssq), false, false);
    ssq = __uint_as_float(r2[0]) + __uint_as_float(r2[1]);
    if (hh == 0) stats[(wv >> 1) * 64 + (wv & 1) * 32 + col] = make_float2(ssum, ssq);
  }
  const int t2 = wv >> 1, hf2 = wv & 1;
  const int c2 = t2 & 1;
  bf16x8 res;
  {
    const unsigned r0 = 8 * hh + q, r1 = 8 * hh + 4 + q, sw = ((q >> 1) & 1) << 2;
    const unsigned chk = (4 * c2 + 2 * blk + (p >> 1)) ^ sw;
    res = k3_bfrag(smem, 128 * r0 + 16 * chk + 8 * (p & 1), 128 * r1 + 16 * chk + 8 * (p & 1), (j * 4 + (t2 >> 1) * 2 + hf2) * 2048);
  }
  __syncthreads();
  float sdv[2], c1v[2], c2v[2];
#pragma unroll
  for (int it = 0; it < 2; ++it) {
    const unsigned c = (wv & 1) ^ it;
    const float2 s0 = stats[c * 32 + col], s1 = stats[64 + c * 32 + col], s2_ = stats[128 + c * 32 + col], s3 = stats[192 + c * 32 + col];
    const float mean = ((s0.x + s1.x) + (s2_.x + s3.x)) * (1.f / D_);
    const float var = fmaxf(__builtin_fmaf(-mean, mean, ((s0.y + s1.y) + (s2_.y + s3.y)) * (1.f / D_)), 0.f);
    const float sd = rbf(sqrtf(rbf(rbf(var) + 1e-5f)));
    const float rs = __builtin_amdgcn_rcpf(sd);
    sdv[it] = sd;
    c1v[it] = rs * -2.3022082f;
    c2v[it] = rs * rs * rs * -0.10294324f;
    const unsigned w0 = pack_bf16(-mean, -mean), w1 = pack_bf16(sd, 0.f);
    const u32x4 bw = {hh ? 0u : w0, hh ? 0u : w1, 0u, 0u};
    acc[it] = __builtin_amdgcn_mfma_f32_32x32x16_bf16(Aext, __builtin_bit_cast(bf16x8, bw), acc[it], 0, 0, 0);
  }
  const __amdgpu_buffer_rsrc_t h1rs = __builtin_amdgcn_make_buffer_rsrc((void*)(ws + OFF_H1), 0, L_ * F_ * 2, 0x00020000);
  u32x4 own[2][2];
#pragma unroll
  for (int it = 0; it < 2; ++it) {
    const unsigned c = (wv & 1) ^ it;
    const unsigned fb = (((grp * 2 + c) * 112 + 2 * ft) * 64 + lane) * 16;
    const f32x2 c1p = {c1v[it], c1v[it]}, c2p = {c2v[it], c2v[it]}, sdp = {sdv[it], sdv[it]};
#pragma unroll
    for (int hf = 0; hf < 2; ++hf) {
      f32x2 a[4], z[4];
#pragma unroll
      for (int e = 0; e < 4; ++e) a[e] = (f32x2){acc[it][8 * hf + 2 * e], acc[it][8 * hf + 2 * e + 1]};
#pragma unroll
      for (int e = 0; e < 4; ++e) z[e] = a[e] * a[e];
#pragma unroll
      for (int e = 0; e < 4; ++e) z[e] = __builtin_elementwise_fma(z[e], c2p, c1p);
#pragma unroll
      for (int e = 0; e < 4; ++e) z[e] = a[e] * z[e];
#pragma unroll
      for (int e = 0; e < 4; ++e) z[e] = (f32x2){__builtin_amdgcn_exp2f(z[e].x), __builtin_amdgcn_exp2f(z[e].y)};
#pragma unroll
      for (int e = 0; e < 4; ++e) z[e] = __builtin_elementwise_fma(z[e], sdp, sdp);
#pragma unroll
      for (int e = 0; e < 4; ++e) z[e] = (f32x2){__builtin_amdgcn_rcpf(z[e].x), __builtin_amdgcn_rcpf(z[e].y)};
#pragma unroll
      for (int e = 0; e < 4; ++e) z[e] = a[e] * z[e];
      const u32x4 pk = {pack_bf16(z[0].x, z[0].y), pack_bf16(z[1].x, z[1].y), pack_bf16(z[2].x, z[2].y), pack_bf16(z[3].x, z[3].y)};
      __builtin_amdgcn_raw_buffer_store_b128(pk, h1rs, fb + hf * 1024, 0, 16);
      own[it][hf] = pk;
      SCHED_FENCE();
    }
  }
  f32x16 acc2[4] = {};
#pragma unroll
  for (int ks = 0; ks < 2; ++ks) {
    const bf16x8 y0 = __builtin_bit_cast(bf16x8, own[0][ks]), y1 = __builtin_bit_cast(bf16x8, own[1][ks]);
    acc2[0] = __builtin_amdgcn_mfma_f32_32x32x16_bf16(a0[ks], y0, acc2[0], 0, 0, 0);
    acc2[1] = __builtin_amdgcn_mfma_f32_32x32x16_bf16(a0[ks], y1, acc2[1], 0, 0, 0);
    acc2[2] = __builtin_amdgcn_mfma_f32_32x32x16_bf16(a1[ks], y0, acc2[2], 0, 0, 0);
    acc2[3] = __builtin_amdgcn_mfma_f32_32x32x16_bf16(a1[ks], y1, acc2[3], 0, 0, 0);
  }
  wait_vm<0>();
  __builtin_amdgcn_s_barrier();
  asm volatile("" ::: "memory");
  gu32* cnt = (gu32*)(ws + OFF_CNT) + grp * 64;
  if (wv == 0) {
    if (lane == 0) {
      __hip_atomic_fetch_add(cnt, 1u, __ATOMIC_RELAXED, __HIP_MEMORY_SCOPE_AGENT);
      for (unsigned spins = 0; spins < (1u << 20); ++spins) {
        if (__hip_atomic_load(cnt, __ATOMIC_RELAXED, __HIP_MEMORY_SCOPE_AGENT) >= 7u) break;
        __builtin_amdgcn_s_sleep(1);
      }
    }
    __builtin_amdgcn_fence(__ATOMIC_ACQUIRE, "wavefront");
  }
  __builtin_amdgcn_s_barrier();
  __builtin_amdgcn_fence(__ATOMIC_ACQUIRE, "wavefront");
  asm volatile("" ::: "memory");
  const unsigned hb0 = (((grp * 2 + (wv & 1)) * 112) * 64 + lane) * 16, hb1 = (((grp * 2 + 1 - (wv & 1)) * 112) * 64 + lane) * 16;
  u32x4 b0[H1_WIN], b1v[H1_WIN];
#pragma unroll
  for (int i = 0; i < H1_WIN; ++i) {
    b0[i] = __builtin_amdgcn_raw_buffer_load_b128(h1rs, hb0, KSLOT(2 + i) * 1024, H1_AUX);
    b1v[i] = __builtin_amdgcn_raw_buffer_load_b128(h1rs, hb1, KSLOT(2 + i) * 1024, H1_AUX);
  }
  const int d0 = j * 64 + (t2 >> 1) * 32 + 16 * hf2 + 8 * hh;
  const u32x4 bz = *(const u32x4*)(b2 + d0);
  SCHED_FENCE();
#pragma unroll
  for (int ks = 0; ks < 12; ++ks) {
    const int sl = ks % H1_WIN;
    const bf16x8 y0 = __builtin_bit_cast(bf16x8, b0[sl]), y1 = __builtin_bit_cast(bf16x8, b1v[sl]);
    acc2[0] = __builtin_amdgcn_mfma_f32_32x32x16_bf16(a0[2 + ks], y0, acc2[0], 0, 0, 0);
    acc2[1] = __builtin_amdgcn_mfma_f32_32x32x16_bf16(a0[2 + ks], y1, acc2[1], 0, 0, 0);
    acc2[2] = __builtin_amdgcn_mfma_f32_32x32x16_bf16(a1[2 + ks], y0, acc2[2], 0, 0, 0);
    acc2[3] = __builtin_amdgcn_mfma_f32_32x32x16_bf16(a1[2 + ks], y1, acc2[3], 0, 0, 0);
    if (ks + H1_WIN < 12) {
      b0[sl] = __builtin_amdgcn_raw_buffer_load_b128(h1rs, hb0, KSLOT(2 + ks + H1_WIN) * 1024, H1_AUX);
      b1v[sl] = __builtin_amdgcn_raw_buffer_load_b128(h1rs, hb1, KSLOT(2 + ks + H1_WIN) * 1024, H1_AUX);
    }
    SCHED_FENCE();
  }
  f32x4* red = (f32x4*)smem;
  f32x4 own0 = {}, own1 = {};
#pragma unroll
  for (int a = 0; a < 8; ++a) {
    const int u = (a ^ (2 * (wv & 1)));
    const f32x4 v0 = {acc2[a >> 1][8 * (a & 1)], acc2[a >> 1][8 * (a & 1) + 1], acc2[a >> 1][8 * (a & 1) + 2], acc2[a >> 1][8 * (a & 1) + 3]};
    const f32x4 v1 = {acc2[a >> 1][8 * (a & 1) + 4], acc2[a >> 1][8 * (a & 1) + 5], acc2[a >> 1][8 * (a & 1) + 6], acc2[a >> 1][8 * (a & 1) + 7]};
    if (u == wv) { own0 = v0; own1 = v1; }
    else {
      const int slot = u * 7 + (wv > u ? wv - 1 : wv);
      red[(slot * 2) * 64 + lane] = v0;
      red[(slot * 2 + 1) * 64 + lane] = v1;
    }
  }
  __syncthreads();
  {
    f32x4 s0[7], s1[7];
#pragma unroll
    for (int i = 0; i < 7; ++i) { s0[i] = red[((wv * 7 + i) * 2) * 64 + lane]; s1[i] = red[((wv * 7 + i) * 2 + 1) * 64 + lane]; }
    const f32x4 o0 = ((own0 + s0[0]) + (s0[1] + s0[2])) + ((s0[3] + s0[4]) + (s0[5] + s0[6]));
    const f32x4 o1 = ((own1 + s1[0]) + (s1[1] + s1[2])) + ((s1[3] + s1[4]) + (s1[5] + s1[6]));
    const u32x4 hr = __builtin_bit_cast(u32x4, res);
    unsigned pk[4];
#pragma unroll
    for (int e = 0; e < 4; ++e) {
      const float x0 = (e < 2) ? o0[2 * e] : o1[2 * e - 4], x1 = (e < 2) ? o0[2 * e + 1] : o1[2 * e - 3];
      const unsigned ob_ = pack_bf16(x0, x1);
      const unsigned o2 = pack_bf16(bflo(ob_) + bflo(bz[e]), bfhi(ob_) + bfhi(bz[e]));
      pk[e] = pack_bf16(bflo(hr[e]) + bflo(o2), bfhi(hr[e]) + bfhi(o2));
    }
    const __amdgpu_buffer_rsrc_t ors = __builtin_amdgcn_make_buffer_rsrc((void*)out, 0, L_ * D_ * 2, 0x00020000);
    __builtin_amdgcn_raw_buffer_store_b128((u32x4){pk[0], pk[1], pk[2], pk[3]}, ors, ((tok0 + c2 * 32 + col) * D_ + d0) * 2, 0, OUT_AUX);
  }
}

extern "C" void kernel_launch(void* const* d_in, const int* in_sizes, int n_in, void* d_out, int out_size, void* d_ws,
                              size_t ws_size, hipStream_t stream) {
  const u16* x = (const u16*)d_in[0];
  const u16* lar = (const u16*)d_in[1];
  const u16* aim = (const u16*)d_in[2];
  const u16* cr = (const u16*)d_in[3];
  const u16* ci = (const u16*)d_in[4];
  const u16* ldt = (const u16*)d_in[5];
  const u16* dsk = (const u16*)d_in[6];
  const u16* w1 = (const u16*)d_in[8];
  const u16* b1 = (const u16*)d_in[9];
  const u16* w2 = (const u16*)d_in[10];
  const u16* b2 = (const u16*)d_in[11];
  char* ws = (char*)d_ws;
  k1_kernel<<<K1_GRID, 256, 0, stream>>>(x, lar, aim, cr, ci, ldt, w1, b1, w2, ws);
  k2_kernel<<<224, 512, 0, stream>>>(dsk, b2, ws, (u16*)d_out);
}
```

```cpp
#include <hip/hip_runtime.h>
#include <hip/hip_bf16.h>
#include <stdint.h>

typedef __attribute__((ext_vector_type(8))) __bf16 bf16x8;
typedef __attribute__((ext_vector_type(4))) __bf16 bf16x4;
typedef __attribute__((ext_vector_type(2))) __bf16 bf16x2;
typedef __attribute__((ext_vector_type(8))) _Float16 f16x8;
typedef __attribute__((ext_vector_type(2))) _Float16 f16x2;
typedef __attribute__((ext_vector_type(2))) float f32x2;
typedef __attribute__((ext_vector_type(4))) float f32x4;
typedef __attribute__((ext_vector_type(16))) float f32x16;
typedef __attribute__((ext_vector_type(4))) unsigned u32x4;
typedef __attribute__((ext_vector_type(2))) unsigned u32x2;
typedef unsigned short u16;

#define DI __device__ __forceinline__
#define SCHED_FENCE() __builtin_amdgcn_sched_barrier(0)

constexpr int L_ = 2048, D_ = 448, N_ = 256, F_ = 1792;
constexpr int KR_LEN = 2304;

constexpr size_t OFF_UT = 0;
constexpr size_t OFF_XT = OFF_UT + (size_t)D_ * L_ * 2;
constexpr size_t OFF_KR = OFF_XT + (size_t)D_ * L_ * 2;
constexpr size_t OFF_KS = OFF_KR + (size_t)D_ * KR_LEN * 2;
constexpr int W1_FRAGS = 29;
constexpr size_t OFF_CNT = OFF_KS + 2048;
constexpr size_t OFF_W1 = OFF_CNT + 32768;
constexpr size_t OFF_W2 = OFF_W1 + (size_t)56 * W1_FRAGS * 1024;
constexpr int HT_LD = L_ + 128;
constexpr size_t OFF_HT = OFF_W2 + (size_t)D_ * F_ * 2;
constexpr size_t OFF_H1 = OFF_HT + (size_t)D_ * HT_LD * 2;
constexpr size_t WS_END = OFF_H1 + (size_t)L_ * F_ * 2;

DI float bf2f(u16 v) { return __uint_as_float(((unsigned)v) << 16); }
DI float bflo(unsigned p) { return __uint_as_float(p << 16); }
DI float bfhi(unsigned p) { return __uint_as_float(p & 0xffff0000u); }
DI unsigned pack_bf16(float a, float b) {
  f32x2 v = {a, b};
  return __builtin_bit_cast(unsigned, __builtin_convertvector(v, bf16x2));
}
DI unsigned pack_f16(float a, float b) {
  f32x2 v = {a, b};
  return __builtin_bit_cast(unsigned, __builtin_convertvector(v, f16x2));
}
DI float rbf(float a) { return bflo(pack_bf16(a, a)); }
template <int CTRL> DI float dpp_add(float v) {
  const int t = __builtin_amdgcn_update_dpp(0, __float_as_int(v), CTRL, 0xf, 0xf, true);
  return v + __int_as_float(t);
}
DI float wave_sum(float v) {
  v = dpp_add<0xB1>(v);
  v = dpp_add<0x4E>(v);
  v = dpp_add<0x141>(v);
  v = dpp_add<0x140>(v);
  auto r = __builtin_amdgcn_permlane16_swap(__float_as_uint(v), __float_as_uint(v), false, false);
  v = __uint_as_float(r[0]) + __uint_as_float(r[1]);
  auto r2 = __builtin_amdgcn_permlane32_swap(__float_as_uint(v), __float_as_uint(v), false, false);
  return __uint_as_float(r2[0]) + __uint_as_float(r2[1]);
}
#define LN_AUX 2
#define OUT_AUX 0
DI __amdgpu_buffer_rsrc_t ws_rsrc(char* ws) { return __builtin_amdgcn_make_buffer_rsrc((void*)ws, 0, (int)WS_END, 0x00020000); }
DI void wst128(__amdgpu_buffer_rsrc_t rs, size_t off, u32x4 v) { __builtin_amdgcn_raw_buffer_store_b128(v, rs, (unsigned)off, 0, 16); }
DI f32x2 cswz(f32x2 b) { return b; }
DI f32x2 cm_b(f32x2 t, f32x2 a, f32x2 b) {
  f32x2 o;
  asm("v_pk_fma_f32 %0, %1, %2, %3 op_sel:[1,1,0] op_sel_hi:[1,0,1] neg_lo:[0,1,0]" : "=v"(o) : "v"(a), "v"(b), "v"(t));
  return o;
}
DI f32x2 cexp_p(float ar2, float air, float p) {
  const float mag = __builtin_amdgcn_exp2f(ar2 * p);
  const float ph = __builtin_amdgcn_fractf(air * p);
  return (f32x2){mag * __builtin_amdgcn_cosf(ph), mag * __builtin_amdgcn_sinf(ph)};
}
DI unsigned pk16(f32x2 v) { return __builtin_bit_cast(unsigned, __builtin_convertvector(v, f16x2)); }
DI void pow2_scale(float s, float& scale, float& inv) {
  int ex = (s > 0.f) ? __builtin_amdgcn_frexp_expf(s) : 14;
  ex = min(max(ex, -100), 120);
  scale = ldexpf(1.f, 14 - ex);
  inv = ldexpf(1.f, ex - 14);
}
DI int perm23(int r) { return (r & ~12) | ((r & 4) << 1) | ((r & 8) >> 1); }

constexpr int K1_KGEN = 448, K1_LN = 128, K1_WT = 112;
constexpr int K1_GRID = K1_KGEN + K1_LN + K1_WT;
constexpr int KG_RS = 272;
constexpr int KG_WAVE = 32 * KG_RS;
constexpr int KG_PART = 4 * KG_WAVE;
constexpr int K1_LDS = KG_PART + 4 * 48 * 16 * 4 + 64;

struct KTab { unsigned short v[2048]; };
constexpr KTab make_ktab() {
  KTab t{};
  for (int i = 0; i < 2048; ++i) {
    const int l = 2047 - i;
    int e = 0;
    while ((l >> (8 + e)) != 0) ++e;
    int p = l;
    if (e > 0) {
      const int q = l >> e, rem = l & ((1 << e) - 1), half = 1 << (e - 1);
      p = (q + ((rem > half || (rem == half && (q & 1))) ? 1 : 0)) << e;
    }
    int e2 = 0;
    while ((p >> (8 + e2)) != 0) ++e2;
    if (e2 > 3) e2 = 3;
    const int row = 8 * e2 + ((p >> 7) << (3 - e2)) + ((p & 7) >> e2);
    const int col = (p & 127) >> 3;
    t.v[i] = (unsigned short)(row * 16 + col);
  }
  return t;
}
__device__ const KTab KTAB = make_ktab();

DI void k1_kgen(char* smem, int d, const u16* __restrict__ lar_p, const u16* __restrict__ aim_p,
                const u16* __restrict__ cr_p, const u16* __restrict__ ci_p, const u16* __restrict__ ldt_p,
                char* __restrict__ ws) {
  const int tid = threadIdx.x, lane = tid & 63;
  const int kq = __builtin_amdgcn_readfirstlane(tid >> 6);
  const int idx = d * N_ + tid;
  const float lar = bf2f(lar_p[idx]), aim = bf2f(aim_p[idx]), cr = bf2f(cr_p[idx]), ci = bf2f(ci_p[idx]);
  const float ldt = bf2f(ldt_p[d]);
  const u32x4 tab = *(const u32x4*)(&KTAB.v[tid * 8]);
  const float LOG2E = 1.4426950408889634f;
  const float dt = rbf(__builtin_amdgcn_exp2f(ldt * LOG2E));
  const float are = -rbf(__builtin_amdgcn_exp2f(lar * LOG2E));
  const float ar = are * dt, ai = aim * dt;
  const float Cr = cr * dt, Ci = ci * dt;
  float* red = (float*)(smem + K1_LDS - 64);
  const float sw = wave_sum(2.f * (fabsf(Cr) + fabsf(Ci)));
  float wscale, winv;
  pow2_scale(sw, wscale, winv);
  if (lane == 0) red[kq] = sw;
  const float ar2 = ar * LOG2E, air = ai * 0.15915494309189535f;
  char* wb = smem + kq * KG_WAVE + lane * 4;
  char* hb = wb + 16 * KG_RS;
  const int r16 = lane & 15, gq = lane >> 4;
  const char* base = smem + kq * KG_WAVE + r16 * KG_RS + gq * 16;
  f32x4 acc[3] = {};
  f16x8 bfr[4];
#define CM_A(t, a, b) const f32x2 t = (f32x2){(a).x, (a).x} * (b)
#define CM_B(t, a, bs) cm_b(t, a, bs)
  f32x2 Lo[16], g[8], R[8], z[9];
  const f32x2 E1 = cexp_p(ar2, air, 1.f);
  Lo[1] = cexp_p(ar2, -air, 8.f);
  R[1] = cexp_p(ar2, air, 128.f);
  g[0] = (f32x2){2.f * wscale * Cr, 2.f * wscale * Ci};
  const f32x2 g0s = cswz(g[0]), R1s = cswz(R[1]);
  f32x2 E2, E4, R8, R16;
  {
    const f32x2 E1s = cswz(E1), L1s = cswz(Lo[1]);
    CM_A(t0, E1, E1); CM_A(t1, Lo[1], Lo[1]); CM_A(t2, R[1], R[1]); CM_A(t3, E1, g[0]);
    SCHED_FENCE();
    E2 = CM_B(t0, E1, E1s); Lo[2] = CM_B(t1, Lo[1], L1s); R[2] = CM_B(t2, R[1], R1s); g[1] = CM_B(t3, E1, g0s);
    SCHED_FENCE();
  }
  const f32x2 E2s = cswz(E2), L2s = cswz(Lo[2]), R2s = cswz(R[2]);
  {
    CM_A(t0, E2, E2); CM_A(t1, Lo[2], Lo[2]); CM_A(t2, R[2], R[2]); CM_A(t3, g[0], E2); CM_A(t4, g[1], E2);
    CM_A(t5, Lo[1], Lo[2]); CM_A(t6, R[1], R[2]);
    SCHED_FENCE();
    E4 = CM_B(t0, E2, E2s); Lo[4] = CM_B(t1, Lo[2], L2s); R[4] = CM_B(t2, R[2], R2s); g[2] = CM_B(t3, g[0], E2s);
    g[3] = CM_B(t4, g[1], E2s); Lo[3] = CM_B(t5, Lo[1], L2s); R[3] = CM_B(t6, R[1], R2s);
    SCHED_FENCE();
  }
  const f32x2 E4s = cswz(E4), L4s = cswz(Lo[4]), R4s = cswz(R[4]);
  {
    f32x2 t[12];
#pragma unroll
    for (int k = 0; k < 4; ++k) {
      t[k] = (f32x2){g[k].x, g[k].x} * E4;
      t[4 + k] = (f32x2){Lo[k + 1].x, Lo[k + 1].x} * Lo[4];
      t[8 + k] = (f32x2){R[k + 1].x, R[k + 1].x} * R[4];
    }
    SCHED_FENCE();
#pragma unroll
    for (int k = 0; k < 4; ++k) {
      g[4 + k] = CM_B(t[k], g[k], E4s);
      Lo[5 + k] = CM_B(t[4 + k], Lo[k + 1], L4s);
      if (k < 3) R[5 + k] = CM_B(t[8 + k], R[k + 1], R4s); else R8 = CM_B(t[8 + k], R[4], R4s);
    }
    SCHED_FENCE();
  }
  {
    const f32x2 L8s = cswz(Lo[8]), R8s = cswz(R8), R3s = cswz(R[3]), g4s = cswz(g[4]);
    f32x2 t[33];
#pragma unroll
    for (int k = 1; k < 8; ++k) t[24 + k] = (f32x2){Lo[k].x, Lo[k].x} * Lo[8];
    t[24] = (f32x2){g[0].x, g[0].x} * R8;
    t[32] = (f32x2){R8.x, R8.x} * R8;
#pragma unroll
    for (int c = 0; c < 8; ++c) t[c] = (f32x2){g[c].x, g[c].x} * R[1];
#pragma unroll
    for (int c = 0; c < 4; ++c) { t[8 + c] = (f32x2){g[2 * c].x, g[2 * c].x} * R[2]; t[12 + c] = (f32x2){g[2 * c].x, g[2 * c].x} * R[3]; }
#pragma unroll
    for (int h = 0; h < 4; ++h) { t[16 + 2 * h] = (f32x2){R[4 + h].x, R[4 + h].x} * g[0]; t[17 + 2 * h] = (f32x2){R[4 + h].x, R[4 + h].x} * g[4]; }
    SCHED_FENCE();
    z[0] = CM_B(t[24], g[0], R8s);
    R16 = CM_B(t[32], R8, R8s);
#pragma unroll
    for (int k = 1; k < 8; ++k) Lo[8 + k] = CM_B(t[24 + k], Lo[k], L8s);
#pragma unroll
    for (int c = 0; c < 8; ++c) t[c] = CM_B(t[c], g[c], R1s);
#pragma unroll
    for (int c = 0; c < 4; ++c) { t[8 + c] = CM_B(t[8 + c], g[2 * c], R2s); t[12 + c] = CM_B(t[12 + c], g[2 * c], R3s); }
#pragma unroll
    for (int h = 0; h < 4; ++h) { t[16 + 2 * h] = CM_B(t[16 + 2 * h], R[4 + h], g0s); t[17 + 2 * h] = CM_B(t[17 + 2 * h], R[4 + h], g4s); }
    SCHED_FENCE();
    *(unsigned*)(wb) = 0x00003c00u;
#pragma unroll
    for (int k = 1; k < 16; ++k) *(unsigned*)(wb + k * KG_RS) = pk16(Lo[k]);
#pragma unroll
    for (int c = 0; c < 8; ++c) *(unsigned*)(hb + c * KG_RS) = pk16(g[c]);
#pragma unroll
    for (int i = 0; i < 8; ++i) *(unsigned*)(hb + (8 + i) * KG_RS) = pk16(t[i]);
#pragma unroll
    for (int ks = 0; ks < 4; ++ks) {
      bfr[ks] = *(const f16x8*)(base + ks * 64);
      acc[0] = __builtin_amdgcn_mfma_f32_16x16x32_f16(*(const f16x8*)(base + 16 * KG_RS + ks * 64), bfr[ks], acc[0], 0, 0, 0);
    }
#pragma unroll
    for (int i = 0; i < 16; ++i) *(unsigned*)(hb + i * KG_RS) = pk16(t[8 + i]);
#pragma unroll
    for (int ks = 0; ks < 4; ++ks)
      acc[1] = __builtin_amdgcn_mfma_f32_16x16x32_f16(*(const f16x8*)(base + 16 * KG_RS + ks * 64), bfr[ks], acc[1], 0, 0, 0);
    SCHED_FENCE();
  }
  {
    const f32x2 z0s = cswz(z[0]);
    f32x2 t[9];
#pragma unroll
    for (int k = 1; k < 8; ++k) t[k] = (f32x2){R[k].x, R[k].x} * z[0];
    t[8] = (f32x2){R16.x, R16.x} * g[0];
    SCHED_FENCE();
#pragma unroll
    for (int k = 1; k < 8; ++k) z[k] = CM_B(t[k], R[k], z0s);
    z[8] = CM_B(t[8], R16, g0s);
    SCHED_FENCE();
#pragma unroll
    for (int h = 0; h < 9; ++h) *(unsigned*)(hb + h * KG_RS) = pk16(z[h]);
#pragma unroll
    for (int ks = 0; ks < 4; ++ks)
      acc[2] = __builtin_amdgcn_mfma_f32_16x16x32_f16(*(const f16x8*)(base + 16 * KG_RS + ks * 64), bfr[ks], acc[2], 0, 0, 0);
  }
  {
    float* part = (float*)(smem + KG_PART) + kq;
#pragma unroll
    for (int m = 0; m < 3; ++m)
#pragma unroll
      for (int j = 0; j < 4; ++j) part[((16 * m + 4 * gq + j) * 16 + r16) * 4] = acc[m][j] * winv;
  }
  __syncthreads();
  {
    float scale, inv_scale;
    pow2_scale((red[0] + red[1]) + (red[2] + red[3]), scale, inv_scale);
    const f32x4* part = (const f32x4*)(smem + KG_PART);
    unsigned pk[4];
#pragma unroll
    for (int j = 0; j < 4; ++j) {
      const f32x4 p0 = part[tab[j] & 0xffffu], p1 = part[tab[j] >> 16];
      pk[j] = pack_f16(((p0[0] + p0[1]) + (p0[2] + p0[3])) * scale, ((p1[0] + p1[1]) + (p1[2] + p1[3])) * scale);
    }
    const __amdgpu_buffer_rsrc_t rs = ws_rsrc(ws);
    const size_t kr = OFF_KR + (size_t)d * KR_LEN * 2;
    wst128(rs, kr + tid * 16, (u32x4){pk[0], pk[1], pk[2], pk[3]});
    if (tid < 32) wst128(rs, kr + 4096 + tid * 16, (u32x4){0u, 0u, 0u, 0u});
    if (tid == 0) ((float*)(ws + OFF_KS))[d] = inv_scale;
  }
}

DI float reduce4(const float (&a)[4]) {
  float v[2];
#pragma unroll
  for (int i = 0; i < 2; ++i) {
    auto r = __builtin_amdgcn_permlane32_swap(__float_as_uint(a[i]), __float_as_uint(a[i + 2]), false, false);
    v[i] = __uint_as_float(r[0]) + __uint_as_float(r[1]);
  }
  auto r = __builtin_amdgcn_permlane16_swap(__float_as_uint(v[0]), __float_as_uint(v[1]), false, false);
  float x = __uint_as_float(r[0]) + __uint_as_float(r[1]);
  x = dpp_add<0xB1>(x);
  x = dpp_add<0x4E>(x);
  x = dpp_add<0x141>(x);
  x = dpp_add<0x140>(x);
  return x;
}
DI float bcast_row(float w, int t) { return __uint_as_float(__builtin_amdgcn_readlane(__float_as_uint(w), 16 * t)); }

constexpr int LN_PL = 448 * 32 + 56 * 16;
DI void k1_ln(char* smem, int blk, const u16* __restrict__ x, char* __restrict__ ws) {
  const int tid = threadIdx.x, lane = tid & 63, wv = tid >> 6;
  const int tok0 = blk * 16 + wv * 4;
  const bool act = lane < 56;
  u32x4 xr[4];
#pragma unroll
  for (int t = 0; t < 4; ++t) {
    xr[t] = (u32x4){0u, 0u, 0u, 0u};
    if (act) xr[t] = *(const u32x4*)(x + (size_t)(tok0 + t) * D_ + lane * 8);
  }
  f32x2 xf[4][4];
  float part[4];
#pragma unroll
  for (int t = 0; t < 4; ++t) {
#pragma unroll
    for (int j = 0; j < 4; ++j) xf[t][j] = (f32x2){bflo(xr[t][j]), bfhi(xr[t][j])};
    const f32x2 s2 = (xf[t][0] + xf[t][1]) + (xf[t][2] + xf[t][3]);
    part[t] = s2.x + s2.y;
  }
  const float wm = reduce4(part) * (1.f / D_);
#pragma unroll
  for (int t = 0; t < 4; ++t) {
    const float m = bcast_row(wm, t);
    f32x2 q2 = {0.f, 0.f};
#pragma unroll
    for (int j = 0; j < 4; ++j) { const f32x2 dd = xf[t][j] - (f32x2){m, m}; q2 = __builtin_elementwise_fma(dd, dd, q2); }
    part[t] = act ? q2.x + q2.y : 0.f;
  }
  const float wq = reduce4(part);
  const float sd = rbf(sqrtf(rbf(rbf(wq * (1.f / D_)) + 1e-5f)));
  const float wr = 1.f / sd;
  const float wb = rbf(wm);
  f32x2 un[4][4];
#pragma unroll
  for (int t = 0; t < 4; ++t) {
    const float mb = bcast_row(wb, t), ri = bcast_row(wr, t);
#pragma unroll
    for (int j = 0; j < 4; ++j) {
      const f32x2 dd = xf[t][j] - (f32x2){mb, mb};
      const unsigned pb = pack_bf16(dd.x, dd.y);
      un[t][j] = (f32x2){bflo(pb), bfhi(pb)} * (f32x2){ri, ri};
    }
  }
  if (act) {
    char* ub = smem + lane * (8 * 32 + 16) + wv * 8;
#pragma unroll
    for (int j = 0; j < 8; ++j) {
      u32x2 pu, px;
#pragma unroll
      for (int t = 0; t < 2; ++t) {
        pu[t] = pack_bf16(un[2 * t][j >> 1][j & 1], un[2 * t + 1][j >> 1][j & 1]);
        const unsigned a = xr[2 * t][j >> 1], b = xr[2 * t + 1][j >> 1];
        px[t] = (j & 1) ? __builtin_amdgcn_perm(b, a, 0x07060302u) : __builtin_amdgcn_perm(b, a, 0x05040100u);
      }
      *(u32x2*)(ub + j * 32) = pu;
      *(u32x2*)(ub + LN_PL + j * 32) = px;
    }
  }
  __syncthreads();
  const __amdgpu_buffer_rsrc_t wrs = ws_rsrc(ws);
#pragma unroll
  for (int i = 0; i < 4; ++i) {
    const int ch = i * 128 + (tid >> 1), v = tid & 1;
    if (ch < D_) {
      const int o = ch * 32 + (ch >> 3) * 16 + v * 16;
      const u32x4 a = *(const u32x4*)(smem + o);
      const u32x4 b = *(const u32x4*)(smem + LN_PL + o);
      __builtin_amdgcn_raw_buffer_store_b128(a, wrs, (unsigned)(OFF_UT + ((size_t)ch * L_ + blk * 16 + v * 8) * 2), 0, LN_AUX);
      __builtin_amdgcn_raw_buffer_store_b128(b, wrs, (unsigned)(OFF_XT + ((size_t)ch * L_ + blk * 16 + v * 8) * 2), 0, LN_AUX);
    }
  }
}

typedef __attribute__((address_space(3))) bf16x4* lds_bf16x4_p;
DI void k1_wtile(char* smem, const u16* __restrict__ src, int ldn, size_t dsto, const u16* __restrict__ b1s, char* ws) {
  const int tid = threadIdx.x, lane = tid & 63, wv = tid >> 6;
  const __amdgpu_buffer_rsrc_t rs = ws_rsrc(ws);
  u32x4 v[7];
#pragma unroll
  for (int i = 0; i < 7; ++i) {
    const int c = i * 256 + tid, k = c >> 2, q = c & 3;
    v[i] = *(const u32x4*)(src + (size_t)k * ldn + q * 8);
  }
  unsigned b1v = 0;
  if (b1s) b1v = b1s[perm23(tid & 31)];
  SCHED_FENCE();
  {
    const int q = tid & 3;
    char* dst = smem + (tid >> 2) * 64 + 32 * (q >> 1) + 8 * (q & 1);
#pragma unroll
    for (int i = 0; i < 7; ++i) {
      *(u32x2*)(dst + i * 4096) = (u32x2){v[i][0], v[i][1]};
      *(u32x2*)(dst + i * 4096 + 16) = (u32x2){v[i][2], v[i][3]};
    }
  }
  __syncthreads();
  {
    const int hh = lane >> 5, blk = (lane >> 4) & 1, q = (lane & 15) >> 2, p = lane & 3;
    const char* rd = smem + (8 * hh + q) * 64 + 32 * blk + 8 * p + wv * 1024;
#pragma unroll
    for (int i = 0; i < 7; ++i) {
      const bf16x4 lo = __builtin_amdgcn_ds_read_tr16_b64_v4bf16((lds_bf16x4_p)(rd + i * 4096));
      const bf16x4 hi = __builtin_amdgcn_ds_read_tr16_b64_v4bf16((lds_bf16x4_p)(rd + i * 4096 + 256));
      const bf16x8 f = __builtin_shufflevector(lo, hi, 0, 1, 2, 3, 4, 5, 6, 7);
      wst128(rs, dsto + (size_t)((i * 4 + wv) * 64 + lane) * 16, __builtin_bit_cast(u32x4, f));
    }
  }
  if (b1s) {
    const int cp = tid & 15, kg = tid >> 4;
    f32x2 sacc = {0.f, 0.f};
#pragma unroll
    for (int k = 0; k < 28; ++k) {
      const unsigned w = *(const unsigned*)(smem + (kg * 28 + k) * 64 + cp * 4);
      sacc += (f32x2){bflo(w), bfhi(w)};
    }
    f32x2* red = (f32x2*)(smem + 448 * 64);
    red[tid] = sacc;
    __syncthreads();
    if (tid < 64) {
      const float* redf = (const float*)red;
      float a = 0.f;
#pragma unroll
      for (int g = 0; g < 16; ++g) a += redf[g * 32 + (tid & 31)];
      const float hi = rbf(a);
      const unsigned w0 = pack_bf16(hi, a - hi);
      wst128(rs, dsto + (size_t)(28 * 64 + tid) * 16, (u32x4){tid < 32 ? w0 : 0u, tid < 32 ? b1v : 0u, 0u, 0u});
    }
  }
}

__global__ __launch_bounds__(256, 3) void k1_kernel(const u16* __restrict__ x, const u16* __restrict__ lar,
                                                    const u16* __restrict__ aim, const u16* __restrict__ cr,
                                                    const u16* __restrict__ ci, const u16* __restrict__ ldt,
                                                    const u16* __restrict__ w1, const u16* __restrict__ b1,
                                                    const u16* __restrict__ w2, char* __restrict__ ws) {
  __shared__ __attribute__((aligned(16))) char smem[K1_LDS];
  int b = blockIdx.x;
  if (b < K1_LN) {
    if (b == 0 && threadIdx.x < 65) ((unsigned*)(ws + OFF_CNT))[threadIdx.x * 64] = 0u;
    k1_ln(smem, b, x, ws);
    return;
  }
  b -= K1_LN;
  if (b < K1_KGEN) { k1_kgen(smem, b, lar, aim, cr, ci, ldt, ws); return; }
  b -= K1_KGEN;
  if (b < 56) {
    k1_wtile(smem, w1 + b * 32, F_, OFF_W1 + (size_t)b * W1_FRAGS * 1024, b1 + b * 32, ws);
  } else {
    const int dt = (b - 56) >> 2, kc = (b - 56) & 3;
    k1_wtile(smem, w2 + (size_t)kc * 448 * D_ + dt * 32, D_, OFF_W2 + ((size_t)dt * 112 + kc * 28) * 1024, nullptr, ws);
  }
}

constexpr int CV_CS = 4160;
constexpr int CV_KOFF = 8 * CV_CS;
constexpr int CV_CH = CV_KOFF + KR_LEN * 2;
constexpr int K3_TILE = 448 * 128;
constexpr int K3_RED = 8 * 7 * 2048;
constexpr int K3_STAT = K3_RED;
constexpr int K3_LDS = 2 * CV_CH + 2 * 24576;
static_assert(K3_STAT + 4 * 64 * 8 <= K3_LDS && (CV_CH & 15) == 0, "lds");
typedef __attribute__((address_space(1))) unsigned gu32;

DI bf16x8 k3_bfrag(const char* smem, unsigned lane_off0, unsigned lane_off1, int off) {
  const bf16x4 lo = __builtin_amdgcn_ds_read_tr16_b64_v4bf16((lds_bf16x4_p)(smem + lane_off0 + off));
  const bf16x4 hi = __builtin_amdgcn_ds_read_tr16_b64_v4bf16((lds_bf16x4_p)(smem + lane_off1 + off));
  return __builtin_shufflevector(lo, hi, 0, 1, 2, 3, 4, 5, 6, 7);
}
#define W1_EARLY 5
#define H1_AUX 16
#define POLL_SLEEP 1
#define H1_WIN 6
template <int N> DI void wait_vm() { asm volatile("s_waitcnt vmcnt(%0)" ::"i"(N) : "memory"); }

__global__ __launch_bounds__(512, 2) void k2_kernel(const u16* __restrict__ dskip, const u16* __restrict__ b2,
                                                    char* __restrict__ ws, u16* __restrict__ out) {
  __shared__ __attribute__((aligned(16))) char smem[K3_LDS];
  const int tid = threadIdx.x, lane = tid & 63;
  const int wv = __builtin_amdgcn_readfirstlane(tid >> 6);
  const int xcd = blockIdx.x & 7, idx = blockIdx.x >> 3;
  const int grp = 4 * xcd + idx / 7, j = idx % 7;
  const int tok0 = grp * 64, ft = j * 8 + wv;
  const int sh = (wv >> 1) & 1;
  const int s2 = wv >> 2;
  const __amdgpu_buffer_rsrc_t wrs = ws_rsrc(ws);
  const bf16x8* w1f = (const bf16x8*)(ws + OFF_W1) + (size_t)ft * W1_FRAGS * 64 + lane;
  const bf16x8* w1a = w1f + sh * 128;
  const bf16x8* w1b = w1f + (1 - sh) * 128;
  bf16x8 A[28];
#define ISSUEA(c) do { A[4 * (c)] = w1a[(4 * (c)) * 64]; A[4 * (c) + 1] = w1a[(4 * (c) + 1) * 64]; \
    A[4 * (c) + 2] = w1b[(4 * (c)) * 64]; A[4 * (c) + 3] = w1b[(4 * (c) + 1) * 64]; } while (0)
#define LDA(n) ((((n) & 2) == 0) ? w1a[(4 * ((n) >> 2) + ((n) & 1)) * 64] : w1b[(4 * ((n) >> 2) + ((n) & 1)) * 64])
  bf16x8 Aext;
  {
    const int d = 2 * blockIdx.x + (wv >> 2), cw = wv & 3, m0 = tid & 255;
    char* cs = smem + (wv >> 2) * CV_CH;
    const u16* uT = (const u16*)(ws + OFF_UT) + (size_t)d * L_;
    const u16* xT = (const u16*)(ws + OFF_XT) + (size_t)d * L_;
    const char* kr = ws + OFF_KR + (size_t)d * KR_LEN * 2;
    u32x4 c0 = {0u, 0u, 0u, 0u};
    if (m0 > 0) c0 = *(const u32x4*)(uT + (m0 - 1) * 8);
    const u32x4 c1 = *(const u32x4*)(uT + m0 * 8);
    const u32x4 k0 = *(const u32x4*)(kr + m0 * 16);
    u32x4 k1 = {0u, 0u, 0u, 0u};
    if (m0 < 32) k1 = *(const u32x4*)(kr + 4096 + m0 * 16);
    const int c16 = lane & 15, g4 = lane >> 4;
    u32x2 ue[2], xe[2];
#pragma unroll
    for (int i = 0; i < 2; ++i) {
      const int pp = 256 * (2 * cw + i) + 16 * c16 + 4 * g4;
      ue[i] = *(const u32x2*)(uT + pp);
      xe[i] = *(const u32x2*)(xT + pp);
    }
    const float inv_scale = ((const float*)(ws + OFF_KS))[d];
    const float dsk = bf2f(dskip[d]);
    SCHED_FENCE();
    *(u32x4*)(cs + CV_KOFF + m0 * 16) = k0;
    if (m0 < 32) *(u32x4*)(cs + CV_KOFF + 4096 + m0 * 16) = k1;
    if (m0 < 8) *(u32x4*)(cs + m0 * CV_CS) = (u32x4){0u, 0u, 0u, 0u};
    {
      unsigned pq[8];
#pragma unroll
      for (int e = 0; e < 4; ++e) {
        pq[e] = pack_f16(bflo(c0[e]), bfhi(c0[e]));
        pq[4 + e] = pack_f16(bflo(c1[e]), bfhi(c1[e]));
      }
      unsigned qq[7];
#pragma unroll
      for (int i = 0; i < 7; ++i) qq[i] = __builtin_amdgcn_alignbit(pq[i + 1], pq[i], 16);
      char* dst = cs + (m0 + 1) * 16;
#pragma unroll
      for (int rho = 0; rho < 8; ++rho) {
        const int k = (rho + 1) >> 1;
        const u32x4 w = (rho & 1) ? (u32x4){pq[k], pq[k + 1], pq[k + 2], pq[k + 3]} : (u32x4){qq[k], qq[k + 1], qq[k + 2], qq[k + 3]};
        *(u32x4*)(dst + rho * CV_CS) = w;
      }
    }
    __syncthreads();
    f32x4 cacc[8] = {};
    const char* abase = cs + (c16 & 7) * CV_CS + ((c16 & 8) + 8 * g4) * 2;
    const char* bbase = cs + CV_KOFF + (2032 - 16 * c16 + 8 * g4) * 2;
#pragma unroll
    for (int si = 0; si < 2; ++si) {
      const int sg = 2 * cw + si;
      f16x8 CA[8], CB[8];
#pragma unroll
      for (int i = 0; i < 8; ++i) {
        CA[i] = *(const f16x8*)(abase + sg * 64 + i * 512);
        CB[i] = *(const f16x8*)(bbase + sg * 64 - i * 512);
      }
#pragma unroll
      for (int t = 0; t < 8; ++t) {
#pragma unroll
        for (int ia = 0; ia <= t; ++ia)
          cacc[t] = __builtin_amdgcn_mfma_f32_16x16x32_f16(CA[ia], CB[t - ia], cacc[t], 0, 0, 0);
        if (t >= 3) {
          const int n0 = (si * 5 + (t - 3)) * 2;
          if (n0 < 4 * W1_EARLY) { A[n0] = LDA(n0); A[n0 + 1] = LDA(n0 + 1); }
          SCHED_FENCE();
        }
      }
      SCHED_FENCE();
    }
    f32x4* red = (f32x4*)(smem + 2 * CV_CH + (wv >> 2) * 24576);
    f32x4 own[2] = {};
#pragma unroll
    for (int t = 0; t < 8; ++t) {
      if ((t >> 1) == cw) own[t & 1] = cacc[t];
      else red[(t * 3 + (cw > (t >> 1) ? cw - 1 : cw)) * 64 + lane] = cacc[t];
    }
    __syncthreads();
#pragma unroll
    for (int i = 0; i < 2; ++i) {
      const int t = 2 * cw + i;
      const f32x4 sum = (own[i] + red[(t * 3) * 64 + lane]) + (red[(t * 3 + 1) * 64 + lane] + red[(t * 3 + 2) * 64 + lane]);
      float h[4];
#pragma unroll
      for (int e = 0; e < 4; ++e) {
        const float uu = (e & 1) ? bfhi(ue[i][e >> 1]) : bflo(ue[i][e >> 1]);
        const float xx = (e & 1) ? bfhi(xe[i][e >> 1]) : bflo(xe[i][e >> 1]);
        const float y = rbf(sum[e] * inv_scale);
        const float tt = rbf(y + rbf(uu * dsk));
        h[e] = xx + tt;
      }
      __builtin_amdgcn_raw_buffer_store_b64((u32x2){pack_bf16(h[0], h[1]), pack_bf16(h[2], h[3])}, wrs,
                                            (unsigned)(OFF_HT + ((size_t)d * HT_LD + 256 * t + 16 * c16 + 4 * g4) * 2), 0, 16);
    }
  }
  wait_vm<0>();
  __builtin_amdgcn_s_barrier();
  asm volatile("" ::: "memory");
  if (wv == 0) {
    gu32* gc = (gu32*)(ws + OFF_CNT) + 32 * 64;
    unsigned last = 0;
    if (lane == 0) {
      if (__hip_atomic_fetch_add(gc + (blockIdx.x & 15) * 64, 1u, __ATOMIC_RELAXED, __HIP_MEMORY_SCOPE_AGENT) == 13u)
        last = __hip_atomic_fetch_add(gc + 16 * 64, 1u, __ATOMIC_RELAXED, __HIP_MEMORY_SCOPE_AGENT) == 15u;
    }
    if (__builtin_amdgcn_readfirstlane(last)) {
      if (lane < 16) __hip_atomic_store(gc + (17 + lane) * 64, 1u, __ATOMIC_RELAXED, __HIP_MEMORY_SCOPE_AGENT);
    } else if (lane == 0) {
      for (unsigned spins = 0; spins < (1u << 20); ++spins) {
        if (__hip_atomic_load(gc + (17 + (blockIdx.x & 15)) * 64, __ATOMIC_RELAXED, __HIP_MEMORY_SCOPE_AGENT) != 0u) break;
        __builtin_amdgcn_s_sleep(POLL_SLEEP);
      }
    }
    __builtin_amdgcn_fence(__ATOMIC_ACQUIRE, "wavefront");
  }
  SCHED_FENCE();
#pragma unroll
  for (int c = W1_EARLY; c < 7; ++c) ISSUEA(c);
  Aext = w1f[28 * 64];
  SCHED_FENCE();
  __builtin_amdgcn_s_barrier();
  __builtin_amdgcn_fence(__ATOMIC_ACQUIRE, "wavefront");
  asm volatile("" ::: "memory");
  const unsigned hsrc = (unsigned)(OFF_HT + ((size_t)(tid >> 3) * HT_LD + tok0 + (tid & 7) * 8) * 2);
  const __amdgpu_buffer_rsrc_t w2rs = __builtin_amdgcn_make_buffer_rsrc((void*)(ws + OFF_W2), 0, D_ * F_ * 2, 0x00020000);
  const unsigned w2v = ((2 * j) * 112 * 64 + lane) * 16;
#define KSLOT(sl) ((sl) < 2 ? 16 * j + 2 * wv + (sl) : (12 * wv + (sl) - 2) + ((12 * wv + (sl) - 2) >= 16 * j ? 16 : 0))
#define LDW2(sl, dt) __builtin_bit_cast(bf16x8, __builtin_amdgcn_raw_buffer_load_b128(w2rs, w2v + (dt) * 112 * 1024, KSLOT(sl) * 1024, 0))
  u32x4 st[7];
  bf16x8 a0[14], a1[14];
#define ISSUE2(c) do { a0[2 * (c)] = LDW2(2 * (c), 0); a1[2 * (c)] = LDW2(2 * (c), 1); \
    a0[2 * (c) + 1] = LDW2(2 * (c) + 1, 0); a1[2 * (c) + 1] = LDW2(2 * (c) + 1, 1); } while (0)
#pragma unroll
  for (int c = 0; c < 7; ++c) st[c] = __builtin_amdgcn_raw_buffer_load_b128(wrs, hsrc, 64 * c * HT_LD * 2, 16);
  SCHED_FENCE();
  const int hh = lane >> 5, col = lane & 31;
  const unsigned q = (lane & 15) >> 2, p = lane & 3, blk = (lane >> 4) & 1;
  char* lw = smem + 128 * (tid >> 3) + 16 * ((tid & 7) ^ (((tid >> 4) & 1) << 2));
  unsigned oa[2][2], ob[2][2];
  {
    const unsigned r0 = 8 * hh + q, r1 = 8 * hh + 4 + q, sw = ((q >> 1) & 1) << 2;
#pragma unroll
    for (int it = 0; it < 2; ++it) {
      const unsigned c = (wv & 1) ^ it;
      const unsigned chk = (4 * c + 2 * blk + (p >> 1)) ^ sw;
      const unsigned x0 = 128 * r0 + 16 * chk + 8 * (p & 1), x1 = 128 * r1 + 16 * chk + 8 * (p & 1);
      oa[it][0] = x0 + sh * 4096; oa[it][1] = x1 + sh * 4096;
      ob[it][0] = x0 + (1 - sh) * 4096; ob[it][1] = x1 + (1 - sh) * 4096;
    }
  }
  f32x16 acc[2] = {};
  float ssum = 0.f, ssq = 0.f;
  const bf16x2 ones2 = {(__bf16)1.0f, (__bf16)1.0f};
#define WRITE1(c) do { *(u32x4*)(lw + (c) * 8192) = st[c]; } while (0)
#define RDB(n, it) ((((n) & 2) == 0) ? k3_bfrag(smem, oa[it][0], oa[it][1], ((n) >> 2) * 8192 + ((n) & 1) * 2048) \
                                      : k3_bfrag(smem, ob[it][0], ob[it][1], ((n) >> 2) * 8192 + ((n) & 1) * 2048))
  bf16x8 Bf[3][2];
#pragma unroll
  for (int c = 0; c < 7; ++c) WRITE1(c);
  __syncthreads();
  Bf[0][0] = RDB(0, 0); Bf[0][1] = RDB(0, 1); Bf[1][0] = RDB(1, 0); Bf[1][1] = RDB(1, 1);
#pragma unroll
  for (int n = 0; n < 28; ++n) {
    const int c = n >> 2, i = n & 3;
    acc[0] = __builtin_amdgcn_mfma_f32_32x32x16_bf16(A[n], Bf[n % 3][0], acc[0], 0, 0, 0);
    if (n + 2 < 28) Bf[(n + 2) % 3][0] = RDB(n + 2, 0);
    if (i < 2) {
      const u32x4 B = __builtin_bit_cast(u32x4, Bf[n % 3][0]);
      const bf16x2 pr = __builtin_bit_cast(bf16x2, s2 ? B[2] : B[0]);
      ssum = __builtin_amdgcn_fdot2_f32_bf16(pr, ones2, ssum, false);
      ssq = __builtin_amdgcn_fdot2_f32_bf16(pr, pr, ssq, false);
    }
    SCHED_FENCE();
    acc[1] = __builtin_amdgcn_mfma_f32_32x32x16_bf16(A[n], Bf[n % 3][1], acc[1], 0, 0, 0);
    if (n + 2 < 28) Bf[(n + 2) % 3][1] = RDB(n + 2, 1);
    if (i < 2) {
      const u32x4 B = __builtin_bit_cast(u32x4, Bf[n % 3][0]);
      const bf16x2 pr = __builtin_bit_cast(bf16x2, s2 ? B[3] : B[1]);
      ssum = __builtin_amdgcn_fdot2_f32_bf16(pr, ones2, ssum, false);
      ssq = __builtin_amdgcn_fdot2_f32_bf16(pr, pr, ssq, false);
    }
    if (i == 3) ISSUE2(c);
    SCHED_FENCE();
  }
  float2* stats = (float2*)(smem + K3_STAT);
  {
    auto r1 = __builtin_amdgcn_permlane32_swap(__float_as_uint(ssum), __float_as_uint(ssum), false, false);
    ssum = __uint_as_float(r1[0]) + __uint_as_float(r1[1]);
    auto r2 = __builtin_amdgcn_permlane32_swap(__float_as_uint(ssq), __float_as_uint(ssq), false, false);
    ssq = __uint_as_float(r2[0]) + __uint_as_float(r2[1]);
    if (hh == 0) stats[(wv >> 1) * 64 + (wv & 1) * 32 + col] = make_float2(ssum, ssq);
  }
  const int t2 = wv >> 1, hf2 = wv & 1;
  const int c2 = t2 & 1;
  bf16x8 res;
  {
    const unsigned r0 = 8 * hh + q, r1 = 8 * hh + 4 + q, sw = ((q >> 1) & 1) << 2;
    const unsigned chk = (4 * c2 + 2 * blk + (p >> 1)) ^ sw;
    res = k3_bfrag(smem, 128 * r0 + 16 * chk + 8 * (p & 1), 128 * r1 + 16 * chk + 8 * (p & 1), (j * 4 + (t2 >> 1) * 2 + hf2) * 2048);
  }
  __syncthreads();
  float sdv[2], c1v[2], c2v[2];
#pragma unroll
  for (int it = 0; it < 2; ++it) {
    const unsigned c = (wv & 1) ^ it;
    const float2 s0 = stats[c * 32 + col], s1 = stats[64 + c * 32 + col], s2_ = stats[128 + c * 32 + col], s3 = stats[192 + c * 32 + col];
    const float mean = ((s0.x + s1.x) + (s2_.x + s3.x)) * (1.f / D_);
    const float var = fmaxf(__builtin_fmaf(-mean, mean, ((s0.y + s1.y) + (s2_.y + s3.y)) * (1.f / D_)), 0.f);
    const float sd = rbf(__builtin_amdgcn_sqrtf(rbf(rbf(var) + 1e-5f)));
    const float rs = __builtin_amdgcn_rcpf(sd);
    sdv[it] = sd;
    c1v[it] = rs * -2.3022082f;
    c2v[it] = rs * rs * rs * -0.10294324f;
    const unsigned w0 = pack_bf16(-mean, -mean), w1 = pack_bf16(sd, 0.f);
    const u32x4 bw = {hh ? 0u : w0, hh ? 0u : w1, 0u, 0u};
    acc[it] = __builtin_amdgcn_mfma_f32_32x32x16_bf16(Aext, __builtin_bit_cast(bf16x8, bw), acc[it], 0, 0, 0);
  }
  const __amdgpu_buffer_rsrc_t h1rs = __builtin_amdgcn_make_buffer_rsrc((void*)(ws + OFF_H1), 0, L_ * F_ * 2, 0x00020000);
  u32x4 own[2][2];
#pragma unroll
  for (int it = 0; it < 2; ++it) {
    const unsigned c = (wv & 1) ^ it;
    const unsigned fb = (((grp * 2 + c) * 112 + 2 * ft) * 64 + lane) * 16;
    const f32x2 c1p = {c1v[it], c1v[it]}, c2p = {c2v[it], c2v[it]}, sdp = {sdv[it], sdv[it]};
#pragma unroll
    for (int hf = 0; hf < 2; ++hf) {
      f32x2 a[4], z[4];
#pragma unroll
      for (int e = 0; e < 4; ++e) a[e] = (f32x2){acc[it][8 * hf + 2 * e], acc[it][8 * hf + 2 * e + 1]};
#pragma unroll
      for (int e = 0; e < 4; ++e) z[e] = a[e] * a[e];
#pragma unroll
      for (int e = 0; e < 4; ++e) z[e] = __builtin_elementwise_fma(z[e], c2p, c1p);
#pragma unroll
      for (int e = 0; e < 4; ++e) z[e] = a[e] * z[e];
#pragma unroll
      for (int e = 0; e < 4; ++e) z[e] = (f32x2){__builtin_amdgcn_exp2f(z[e].x), __builtin_amdgcn_exp2f(z[e].y)};
#pragma unroll
      for (int e = 0; e < 4; ++e) z[e] = __builtin_elementwise_fma(z[e], sdp, sdp);
#pragma unroll
      for (int e = 0; e < 4; ++e) z[e] = (f32x2){__builtin_amdgcn_rcpf(z[e].x), __builtin_amdgcn_rcpf(z[e].y)};
#pragma unroll
      for (int e = 0; e < 4; ++e) z[e] = a[e] * z[e];
      const u32x4 pk = {pack_bf16(z[0].x, z[0].y), pack_bf16(z[1].x, z[1].y), pack_bf16(z[2].x, z[2].y), pack_bf16(z[3].x, z[3].y)};
      __builtin_amdgcn_raw_buffer_store_b128(pk, h1rs, fb + hf * 1024, 0, 16);
      own[it][hf] = pk;
      SCHED_FENCE();
    }
  }
  f32x16 acc2[4] = {};
#pragma unroll
  for (int ks = 0; ks < 2; ++ks) {
    const bf16x8 y0 = __builtin_bit_cast(bf16x8, own[0][ks]), y1 = __builtin_bit_cast(bf16x8, own[1][ks]);
    acc2[0] = __builtin_amdgcn_mfma_f32_32x32x16_bf16(a0[ks], y0, acc2[0], 0, 0, 0);
    acc2[1] = __builtin_amdgcn_mfma_f32_32x32x16_bf16(a0[ks], y1, acc2[1], 0, 0, 0);
    acc2[2] = __builtin_amdgcn_mfma_f32_32x32x16_bf16(a1[ks], y0, acc2[2], 0, 0, 0);
    acc2[3] = __builtin_amdgcn_mfma_f32_32x32x16_bf16(a1[ks], y1, acc2[3], 0, 0, 0);
  }
  wait_vm<0>();
  __builtin_amdgcn_s_barrier();
  asm volatile("" ::: "memory");
  gu32* cnt = (gu32*)(ws + OFF_CNT) + grp * 64;
  if (wv == 0) {
    if (lane == 0) {
      __hip_atomic_fetch_add(cnt, 1u, __ATOMIC_RELAXED, __HIP_MEMORY_SCOPE_AGENT);
      for (unsigned spins = 0; spins < (1u << 20); ++spins) {
        if (__hip_atomic_load(cnt, __ATOMIC_RELAXED, __HIP_MEMORY_SCOPE_AGENT) >= 7u) break;
        __builtin_amdgcn_s_sleep(1);
      }
    }
    __builtin_amdgcn_fence(__ATOMIC_ACQUIRE, "wavefront");
  }
  __builtin_amdgcn_s_barrier();
  __builtin_amdgcn_fence(__ATOMIC_ACQUIRE, "wavefront");
  asm volatile("" ::: "memory");
  const unsigned hb0 = (((grp * 2 + (wv & 1)) * 112) * 64 + lane) * 16, hb1 = (((grp * 2 + 1 - (wv & 1)) * 112) * 64 + lane) * 16;
  u32x4 b0[H1_WIN], b1v[H1_WIN];
#pragma unroll
  for (int i = 0; i < H1_WIN; ++i) {
    b0[i] = __builtin_amdgcn_raw_buffer_load_b128(h1rs, hb0, KSLOT(2 + i) * 1024, H1_AUX);
    b1v[i] = __builtin_amdgcn_raw_buffer_load_b128(h1rs, hb1, KSLOT(2 + i) * 1024, H1_AUX);
  }
  const int d0 = j * 64 + (t2 >> 1) * 32 + 16 * hf2 + 8 * hh;
  const u32x4 bz = *(const u32x4*)(b2 + d0);
  SCHED_FENCE();
#pragma unroll
  for (int ks = 0; ks < 12; ++ks) {
    const int sl = ks % H1_WIN;
    const bf16x8 y0 = __builtin_bit_cast(bf16x8, b0[sl]), y1 = __builtin_bit_cast(bf16x8, b1v[sl]);
    acc2[0] = __builtin_amdgcn_mfma_f32_32x32x16_bf16(a0[2 + ks], y0, acc2[0], 0, 0, 0);
    acc2[1] = __builtin_amdgcn_mfma_f32_32x32x16_bf16(a0[2 + ks], y1, acc2[1], 0, 0, 0);
    acc2[2] = __builtin_amdgcn_mfma_f32_32x32x16_bf16(a1[2 + ks], y0, acc2[2], 0, 0, 0);
    acc2[3] = __builtin_amdgcn_mfma_f32_32x32x16_bf16(a1[2 + ks], y1, acc2[3], 0, 0, 0);
    if (ks + H1_WIN < 12) {
      b0[sl] = __builtin_amdgcn_raw_buffer_load_b128(h1rs, hb0, KSLOT(2 + ks + H1_WIN) * 1024, H1_AUX);
      b1v[sl] = __builtin_amdgcn_raw_buffer_load_b128(h1rs, hb1, KSLOT(2 + ks + H1_WIN) * 1024, H1_AUX);
    }
    SCHED_FENCE();
  }
  f32x4* red = (f32x4*)smem;
  f32x4 own0 = {}, own1 = {};
#pragma unroll
  for (int a = 0; a < 8; ++a) {
    const int u = (a ^ (2 * (wv & 1)));
    const f32x4 v0 = {acc2[a >> 1][8 * (a & 1)], acc2[a >> 1][8 * (a & 1) + 1], acc2[a >> 1][8 * (a & 1) + 2], acc2[a >> 1][8 * (a & 1) + 3]};
    const f32x4 v1 = {acc2[a >> 1][8 * (a & 1) + 4], acc2[a >> 1][8 * (a & 1) + 5], acc2[a >> 1][8 * (a & 1) + 6], acc2[a >> 1][8 * (a & 1) + 7]};
    if (u == wv) { own0 = v0; own1 = v1; }
    else {
      const int slot = u * 7 + (wv > u ? wv - 1 : wv);
      red[(slot * 2) * 64 + lane] = v0;
      red[(slot * 2 + 1) * 64 + lane] = v1;
    }
  }
  __syncthreads();
  {
    f32x4 s0[7], s1[7];
#pragma unroll
    for (int i = 0; i < 7; ++i) { s0[i] = red[((wv * 7 + i) * 2) * 64 + lane]; s1[i] = red[((wv * 7 + i) * 2 + 1) * 64 + lane]; }
    const f32x4 o0 = ((own0 + s0[0]) + (s0[1] + s0[2])) + ((s0[3] + s0[4]) + (s0[5] + s0[6]));
    const f32x4 o1 = ((own1 + s1[0]) + (s1[1] + s1[2])) + ((s1[3] + s1[4]) + (s1[5] + s1[6]));
    const u32x4 hr = __builtin_bit_cast(u32x4, res);
    unsigned pk[4];
#pragma unroll
    for (int e = 0; e < 4; ++e) {
      const float x0 = (e < 2) ? o0[2 * e] : o1[2 * e - 4], x1 = (e < 2) ? o0[2 * e + 1] : o1[2 * e - 3];
      const unsigned ob_ = pack_bf16(x0, x1);
      const unsigned o2 = pack_bf16(bflo(ob_) + bflo(bz[e]), bfhi(ob_) + bfhi(bz[e]));
      pk[e] = pack_bf16(bflo(hr[e]) + bflo(o2), bfhi(hr[e]) + bfhi(o2));
    }
    const __amdgpu_buffer_rsrc_t ors = __builtin_amdgcn_make_buffer_rsrc((void*)out, 0, L_ * D_ * 2, 0x00020000);
    __builtin_amdgcn_raw_buffer_store_b128((u32x4){pk[0], pk[1], pk[2], pk[3]}, ors, ((tok0 + c2 * 32 + col) * D_ + d0) * 2, 0, OUT_AUX);
  }
}

extern "C" void kernel_launch(void* const* d_in, const int* in_sizes, int n_in, void* d_out, int out_size, void* d_ws,
                              size_t ws_size, hipStream_t stream) {
  const u16* x = (const u16*)d_in[0];
  const u16* lar = (const u16*)d_in[1];
  const u16* aim = (const u16*)d_in[2];
  const u16* cr = (const u16*)d_in[3];
  const u16* ci = (const u16*)d_in[4];
  const u16* ldt = (const u16*)d_in[5];
  const u16* dsk = (const u16*)d_in[6];
  const u16* w1 = (const u16*)d_in[8];
  const u16* b1 = (const u16*)d_in[9];
  const u16* w2 = (const u16*)d_in[10];
  const u16* b2 = (const u16*)d_in[11];
  char* ws = (char*)d_ws;
  k1_kernel<<<K1_GRID, 256, 0, stream>>>(x, lar, aim, cr, ci, ldt, w1, b1, w2, ws);
  k2_kernel<<<224, 512, 0, stream>>>(dsk, b2, ws, (u16*)d_out);
}
```
